```python
import torch

def _softmax(x, axis=-1):
    return torch.softmax(x, dim=axis)
def _gelu(x):
    return torch.nn.functional.gelu(x, approximate="tanh")
def _silu(x):
    return torch.nn.functional.silu(x)
def _sigmoid(x):
    return torch.sigmoid(x)
def _layernorm(x, axis=-1, eps=1e-5):
    m = x.mean(dim=axis, keepdim=True)
    v = x.var(dim=axis, keepdim=True, unbiased=False)
    return (x - m) / torch.sqrt(v + eps)
def _scatter_add(tgt, idx, src, axis=0):
    return tgt.clone().index_add_(axis, idx, src)
def _segment_sum(src, seg, n):
    out = torch.zeros((n,) + src.shape[1:], dtype=src.dtype)
    return out.index_add_(0, seg, src)
def _pad(x, pw):
    flat = tuple(v for pair in reversed(pw) for v in pair)
    return torch.nn.functional.pad(x, flat)

kernel_name = 'sequential_preLN__sliding_block__swiglu_ffn__prompt'

def setup_inputs():

    _w = (lambda a: (lambda t: t.to(torch.bfloat16) if t.is_floating_point() else t)(torch.as_tensor(a)))
    def _A_setup():
        B, S, D = 1, 2048, 448
        return {
            "x": _w((lambda *s: torch.randn(*s, dtype=torch.bfloat16) * 0.05)(B, S, D)),
            "wqkv": _w((lambda *s: torch.randn(*s, dtype=torch.bfloat16) * 0.05)(D, 3 * D)),
            "wo": _w((lambda *s: torch.randn(*s, dtype=torch.bfloat16) * 0.05)(D, D)),
        }
    _A_in = _A_setup()

    def _B_setup():
        B, S, D, F = 1, 2048, 448, 1792
        return {
            "x": _w((lambda *s: torch.randn(*s, dtype=torch.bfloat16) * 0.05)(B, S, D)),
            "w_gate": _w((lambda *s: torch.randn(*s, dtype=torch.bfloat16) * 0.05)(D, F)),
            "w_up": _w((lambda *s: torch.randn(*s, dtype=torch.bfloat16) * 0.05)(D, F)),
            "w_down": _w((lambda *s: torch.randn(*s, dtype=torch.bfloat16) * 0.05)(F, D)),
        }
    _B_in = _B_setup()

    return {
        "A_x": _A_in["x"],
        "A_wqkv": _A_in["wqkv"],
        "A_wo": _A_in["wo"],
        "B_x": _B_in["x"],
        "B_w_gate": _B_in["w_gate"],
        "B_w_up": _B_in["w_up"],
        "B_w_down": _B_in["w_down"],
    }

def reference(A_x, A_wqkv, A_wo, B_x, B_w_gate, B_w_up, B_w_down):
    def _A_ref(x, wqkv, wo):
        B, S, D = x.shape
        H = 16; Dh = D // H; W = 256
        qkv = (lambda x, *d: x.permute(*d))((x @ wqkv).reshape(B, S, 3, H, Dh), 2, 0, 3, 1, 4)
        q, k, v = qkv[0], qkv[1], qkv[2]
        scores = torch.einsum("bhqd,bhkd->bhqk", q, k) * (Dh ** -0.5)
        idx = torch.arange(S)
        mask = (idx[:, None] - idx[None, :] >= 0) & (idx[:, None] - idx[None, :] < W)
        scores = torch.where(mask[None, None], scores, -1e9)
        p = _softmax(scores, axis=-1)
        o = torch.einsum("bhqk,bhkd->bhqd", p, v)
        return (lambda x, *d: x.permute(*d))(o, 0, 2, 1, 3).reshape(B, S, D) @ wo

    def _B_ref(x, w_gate, w_up, w_down):
        g = _silu(x @ w_gate)
        u = x @ w_up
        return (g * u) @ w_down

    x = A_x
    h = x + _A_ref(_layernorm(x, axis=-1), A_wqkv, A_wo)
    h2 = h + _B_ref(_layernorm(h, axis=-1), B_w_gate, B_w_up, B_w_down)
    return h2
```

```cpp
#include <hip/hip_runtime.h>
#include <hip/hip_bf16.h>
#include <stdint.h>

typedef __bf16 bf16x2 __attribute__((ext_vector_type(2)));
typedef __bf16 bf16x4 __attribute__((ext_vector_type(4)));
typedef __bf16 bf16x8 __attribute__((ext_vector_type(8)));
typedef float f32x2 __attribute__((ext_vector_type(2)));
typedef float f32x4 __attribute__((ext_vector_type(4)));
typedef float f32x16 __attribute__((ext_vector_type(16)));
typedef unsigned u32x2 __attribute__((ext_vector_type(2)));
typedef unsigned u32x4 __attribute__((ext_vector_type(4)));
#define LAS __attribute__((address_space(3)))
#define DI __device__ __forceinline__

constexpr int S = 2048, D = 448, H = 16, DH = 28, F = 1792;
constexpr size_t Q_OFF = 0;
constexpr size_t K_OFF = Q_OFF + 16 * 128 * 1024;
constexpr size_t V_OFF = K_OFF + 16 * 128 * 1024;
constexpr size_t O_OFF = V_OFF + 16 * 128 * 1024;
constexpr size_t H_OFF = O_OFF + 128 * 16 * 1024;
constexpr size_t A_OFF = H_OFF + 64 * 28 * 1024;

DI unsigned pk2(float a, float b) { f32x2 f = {a, b}; return __builtin_bit_cast(unsigned, __builtin_convertvector(f, bf16x2)); }
DI float bflo(unsigned u) { return __builtin_bit_cast(float, u << 16); }
DI float bfhi(unsigned u) { return __builtin_bit_cast(float, u & 0xffff0000u); }
DI bf16x4 trread(LAS char* p) { return __builtin_amdgcn_ds_read_tr16_b64_v4bf16((LAS bf16x4*)p); }
DI bf16x8 cat(bf16x4 a, bf16x4 b) { return __builtin_shufflevector(a, b, 0, 1, 2, 3, 4, 5, 6, 7); }
DI f32x4 mfma16(bf16x8 a, bf16x8 b, f32x4 c) { return __builtin_amdgcn_mfma_f32_16x16x32_bf16(a, b, c, 0, 0, 0); }
DI f32x16 mfma32(bf16x8 a, bf16x8 b, f32x16 c) { return __builtin_amdgcn_mfma_f32_32x32x16_bf16(a, b, c, 0, 0, 0); }
DI bf16x8 asbf(u32x4 v) { return __builtin_bit_cast(bf16x8, v); }

DI void decode_block(int b, int& g, int& j) { int x = b & 7, loc = b >> 3; g = 2 * x + (loc >> 4); j = loc & 15; }

__global__ void __launch_bounds__(512, 2) k_qkv(const uint16_t* __restrict__ x, const uint16_t* __restrict__ wqkv, char* __restrict__ ws) {
  extern __shared__ __attribute__((aligned(16))) char smem_[];
  LAS char* smem = (LAS char*)smem_;
  int g, j; decode_block(blockIdx.x, g, j);
  const int tid = threadIdx.x, lane = tid & 63, w = tid >> 6, r = lane & 15, q = lane >> 4;
  u32x4 xf[14];
  const uint16_t* xp = x + (size_t)(128 * g + 16 * w + r) * D + 8 * q;
#pragma unroll
  for (int kk = 0; kk < 14; ++kk) xf[kk] = *(const u32x4*)(xp + 32 * kk);
  for (int i = tid; i < 448 * 24; i += 512) {
    int row = i / 24, rem = i % 24, m = rem >> 3, p = rem & 7;
    u32x2 v = {0u, 0u};
    if (p < 7) v = *(const u32x2*)(wqkv + (size_t)row * 1344 + m * 448 + 28 * j + 4 * p);
    *(LAS u32x2*)(smem + 192 * row + 32 * (row >> 3) + 64 * m + 8 * p) = v;
  }
  float s = 0.f;
#pragma unroll
  for (int kk = 0; kk < 14; ++kk)
#pragma unroll
    for (int e = 0; e < 4; ++e) s += bflo(xf[kk][e]) + bfhi(xf[kk][e]);
  s += __shfl_xor(s, 16); s += __shfl_xor(s, 32);
  const float mean = s * (1.f / D);
  float v2 = 0.f;
#pragma unroll
  for (int kk = 0; kk < 14; ++kk)
#pragma unroll
    for (int e = 0; e < 4; ++e) { float a = bflo(xf[kk][e]) - mean, b = bfhi(xf[kk][e]) - mean; v2 += a * a + b * b; }
  v2 += __shfl_xor(v2, 16); v2 += __shfl_xor(v2, 32);
  const float rstd = rsqrtf(v2 * (1.f / D) + 1e-5f);
#pragma unroll
  for (int kk = 0; kk < 14; ++kk)
#pragma unroll
    for (int e = 0; e < 4; ++e) xf[kk][e] = pk2((bflo(xf[kk][e]) - mean) * rstd, (bfhi(xf[kk][e]) - mean) * rstd);
  __syncthreads();
  f32x4 acc[6];
#pragma unroll
  for (int c = 0; c < 6; ++c) acc[c] = f32x4{0.f, 0.f, 0.f, 0.f};
  const int trow = 8 * q + ((lane & 15) >> 2);
  LAS char* wb = smem + 192 * trow + 32 * q + 8 * (lane & 3);
#pragma unroll
  for (int kk = 0; kk < 14; ++kk) {
#pragma unroll
    for (int c = 0; c < 6; ++c) {
      LAS char* p = wb + kk * (192 * 32 + 128) + 32 * c;
      bf16x8 wf = cat(trread(p), trread(p + 4 * 192));
      if (c < 4) acc[c] = mfma16(wf, asbf(xf[kk]), acc[c]);
      else acc[c] = mfma16(asbf(xf[kk]), wf, acc[c]);
    }
  }
  const float qs = 0.18898223650461362f * 1.4426950408889634f;
  const int tile = 8 * g + w;
  u32x4 qo = {pk2(acc[0][0] * qs, acc[0][1] * qs), pk2(acc[0][2] * qs, acc[0][3] * qs), pk2(acc[1][0] * qs, acc[1][1] * qs), pk2(acc[1][2] * qs, acc[1][3] * qs)};
  u32x4 ko = {pk2(acc[2][0], acc[2][1]), pk2(acc[2][2], acc[2][3]), pk2(acc[3][0], acc[3][1]), pk2(acc[3][2], acc[3][3])};
  *(u32x4*)(ws + Q_OFF + ((size_t)j * 128 + tile) * 1024 + lane * 16) = qo;
  *(u32x4*)(ws + K_OFF + ((size_t)j * 128 + tile) * 1024 + lane * 16) = ko;
  u32x2 v0 = {pk2(acc[4][0], acc[4][1]), pk2(acc[4][2], acc[4][3])};
  u32x2 v1 = {pk2(acc[5][0], acc[5][1]), pk2(acc[5][2], acc[5][3])};
  if (r == 12) v1 = u32x2{0x3f803f80u, 0x3f803f80u};
  char* vp = ws + V_OFF + ((size_t)j * 64 + (tile >> 1)) * 2048 + lane * 16 + (tile & 1) * 8;
  *(u32x2*)vp = v0;
  *(u32x2*)(vp + 1024) = v1;
}

__global__ void __launch_bounds__(512, 2) k_attn(char* __restrict__ ws) {
  int g, j; decode_block(blockIdx.x, g, j);
  const int tid = threadIdx.x, lane = tid & 63, w = tid >> 6, r = lane & 15, q = lane >> 4;
  const int tile = 8 * g + w;
  const int t0 = (tile - 16) & ~1;
  bf16x8 qf = asbf(*(const u32x4*)(ws + Q_OFF + ((size_t)j * 128 + tile) * 1024 + lane * 16));
  f32x4 sc[18];
  const int qi = 16 * tile + r;
  float mx = -3.0e38f;
#pragma unroll
  for (int t = 0; t < 18; ++t) {
    int kt = t0 + t; int ktc = kt < 0 ? 0 : (kt > 127 ? 127 : kt);
    bf16x8 kf = asbf(*(const u32x4*)(ws + K_OFF + ((size_t)j * 128 + ktc) * 1024 + lane * 16));
    sc[t] = mfma16(kf, qf, f32x4{0.f, 0.f, 0.f, 0.f});
#pragma unroll
    for (int e = 0; e < 4; ++e) {
      int key = 16 * kt + 4 * q + e; int dlt = qi - key;
      bool ok = (dlt >= 0) && (dlt < 256) && (key >= 0);
      sc[t][e] = ok ? sc[t][e] : -3.0e38f;
      mx = fmaxf(mx, sc[t][e]);
    }
  }
  mx = fmaxf(mx, __shfl_xor(mx, 16)); mx = fmaxf(mx, __shfl_xor(mx, 32));
  f32x4 o0 = {0.f, 0.f, 0.f, 0.f}, o1 = {0.f, 0.f, 0.f, 0.f};
#pragma unroll
  for (int T = 0; T < 9; ++T) {
    float p[8];
#pragma unroll
    for (int e = 0; e < 4; ++e) { p[e] = __builtin_amdgcn_exp2f(sc[2 * T][e] - mx); p[4 + e] = __builtin_amdgcn_exp2f(sc[2 * T + 1][e] - mx); }
    u32x4 pf = {pk2(p[0], p[1]), pk2(p[2], p[3]), pk2(p[4], p[5]), pk2(p[6], p[7])};
    int pt = (t0 >> 1) + T; pt = pt < 0 ? 0 : pt;
    const char* vp = ws + V_OFF + ((size_t)j * 64 + pt) * 2048 + lane * 16;
    bf16x8 vf0 = asbf(*(const u32x4*)vp), vf1 = asbf(*(const u32x4*)(vp + 1024));
    o0 = mfma16(vf0, asbf(pf), o0);
    o1 = mfma16(vf1, asbf(pf), o1);
  }
  float l = __shfl(o1[0], 48 + r);
  float inv = 1.f / l;
  u32x4 of = {pk2(o0[0] * inv, o0[1] * inv), pk2(o0[2] * inv, o0[3] * inv), pk2(o1[0] * inv, o1[1] * inv), pk2(o1[2] * inv, o1[3] * inv)};
  if (q == 3) { of[2] = 0u; of[3] = 0u; }
  *(u32x4*)(ws + O_OFF + ((size_t)tile * 16 + j) * 1024 + lane * 16) = of;
}

__global__ void __launch_bounds__(512, 2) k_wo(const uint16_t* __restrict__ x, const uint16_t* __restrict__ wo, char* __restrict__ ws) {
  extern __shared__ __attribute__((aligned(16))) char smem_[];
  LAS char* smem = (LAS char*)smem_;
  int g, j; decode_block(blockIdx.x, g, j);
  if (j >= 14) return;
  const int th = j / 7, fc = j % 7;
  const int tid = threadIdx.x, lane = tid & 63, w = tid >> 6, r = lane & 15, q = lane >> 4;
  const int tt = w & 3, fh = w >> 2;
  const int tile = 8 * g + 4 * th + tt;
  u32x4 of[16];
#pragma unroll
  for (int hh = 0; hh < 16; ++hh) of[hh] = *(const u32x4*)(ws + O_OFF + ((size_t)tile * 16 + hh) * 1024 + lane * 16);
  for (int i = tid; i < 452 * 8; i += 512) {
    int row = i >> 3, c = i & 7;
    u32x4 v = {0u, 0u, 0u, 0u};
    if (row < 448) v = *(const u32x4*)(wo + (size_t)row * D + 64 * fc + 8 * c);
    *(LAS u32x4*)(smem + 160 * row + 16 * c) = v;
  }
  const int tok = 16 * tile + r, f0 = 64 * fc + 32 * fh;
  u32x2 x0 = *(const u32x2*)(x + (size_t)tok * D + f0 + 4 * q), x1 = *(const u32x2*)(x + (size_t)tok * D + f0 + 16 + 4 * q);
  __syncthreads();
  f32x4 a0 = {0.f, 0.f, 0.f, 0.f}, a1 = {0.f, 0.f, 0.f, 0.f};
  LAS char* wb = smem + 160 * (4 * q + ((lane & 15) >> 2)) + 64 * fh + 8 * (lane & 3);
#pragma unroll
  for (int hh = 0; hh < 16; ++hh) {
    LAS char* p = wb + 160 * 28 * hh;
    bf16x8 w0 = cat(trread(p), trread(p + 16 * 160));
    bf16x8 w1 = cat(trread(p + 32), trread(p + 16 * 160 + 32));
    a0 = mfma16(w0, asbf(of[hh]), a0);
    a1 = mfma16(w1, asbf(of[hh]), a1);
  }
  u32x4 ho = {pk2(a0[0] + bflo(x0[0]), a0[1] + bfhi(x0[0])), pk2(a0[2] + bflo(x0[1]), a0[3] + bfhi(x0[1])),
              pk2(a1[0] + bflo(x1[0]), a1[1] + bfhi(x1[0])), pk2(a1[2] + bflo(x1[1]), a1[3] + bfhi(x1[1]))};
  const int ft = 2 * fc + fh;
  *(u32x4*)(ws + H_OFF + ((size_t)(tile >> 1) * 28 + 2 * ft + (q >> 1)) * 1024 + ((tile & 1) * 16 + r + 32 * (q & 1)) * 16) = ho;
}

__global__ void __launch_bounds__(512, 2) k_gu(const uint16_t* __restrict__ wg, const uint16_t* __restrict__ wu, char* __restrict__ ws) {
  extern __shared__ __attribute__((aligned(16))) char smem_[];
  LAS char* smem = (LAS char*)smem_;
  int g, j; decode_block(blockIdx.x, g, j);
  if (j >= 14) return;
  const int tid = threadIdx.x, lane = tid & 63, w = tid >> 6, hh = lane >> 5;
  bf16x8 wf[28];
  constexpr int WST = 576;
#pragma unroll
  for (int half = 0; half < 2; ++half) {
    for (int i = tid; i < 224 * 32; i += 512) {
      int row = i >> 5, c = i & 31;
      const uint16_t* src = (c < 16 ? wg : wu) + (size_t)(224 * half + row) * F + 128 * j + 8 * (c & 15);
      u32x4 v = *(const u32x4*)src;
      *(LAS u32x4*)(smem + WST * row + 16 * c + (c >= 16 ? 32 : 0)) = v;
    }
    __syncthreads();
    const int blk = (lane >> 4) & 1;
    LAS char* wb = smem + WST * (4 * hh + ((lane & 15) >> 2)) + blk * (256 + 32) + 32 * w + 8 * (lane & 3);
#pragma unroll
    for (int ks = 0; ks < 14; ++ks) {
      LAS char* p = wb + WST * (32 * (ks >> 1) + 8 * (ks & 1));
      wf[14 * half + ks] = cat(trread(p), trread(p + 16 * WST));
    }
    __syncthreads();
  }
  {
    const int tt = w & 3, kh = w >> 2;
    u32x4 hf[14];
#pragma unroll
    for (int ks = 0; ks < 14; ++ks) hf[ks] = *(const u32x4*)(ws + H_OFF + ((size_t)(4 * g + tt) * 28 + 14 * kh + ks) * 1024 + lane * 16);
    LAS float* st = (LAS float*)(smem + 114688);
    float s = 0.f;
#pragma unroll
    for (int ks = 0; ks < 14; ++ks)
#pragma unroll
      for (int e = 0; e < 4; ++e) s += bflo(hf[ks][e]) + bfhi(hf[ks][e]);
    s += __shfl_xor(s, 32);
    st[w * 64 + lane] = s;
    __syncthreads();
    const float mean = (s + st[(w ^ 4) * 64 + lane]) * (1.f / D);
    float v2 = 0.f;
#pragma unroll
    for (int ks = 0; ks < 14; ++ks)
#pragma unroll
      for (int e = 0; e < 4; ++e) { float a = bflo(hf[ks][e]) - mean, b = bfhi(hf[ks][e]) - mean; v2 += a * a + b * b; }
    v2 += __shfl_xor(v2, 32);
    st[512 + w * 64 + lane] = v2;
    __syncthreads();
    const float rstd = rsqrtf((v2 + st[512 + (w ^ 4) * 64 + lane]) * (1.f / D) + 1e-5f);
#pragma unroll
    for (int ks = 0; ks < 14; ++ks) {
      u32x4 o;
#pragma unroll
      for (int e = 0; e < 4; ++e) o[e] = pk2((bflo(hf[ks][e]) - mean) * rstd, (bfhi(hf[ks][e]) - mean) * rstd);
      *(LAS u32x4*)(smem + (tt * 28 + 14 * kh + ks) * 1024 + lane * 16) = o;
    }
    __syncthreads();
  }
#pragma unroll 1
  for (int tt = 0; tt < 4; ++tt) {
    f32x16 acc;
#pragma unroll
    for (int i = 0; i < 16; ++i) acc[i] = 0.f;
#pragma unroll
    for (int ks = 0; ks < 28; ++ks) {
      bf16x8 b = asbf(*(LAS u32x4*)(smem + (tt * 28 + ks) * 1024 + lane * 16));
      acc = mfma32(wf[ks], b, acc);
    }
    float a[8];
#pragma unroll
    for (int e = 0; e < 8; ++e) { float gg = acc[e]; a[e] = gg * __builtin_amdgcn_rcpf(1.f + __expf(-gg)) * acc[8 + e]; }
    u32x4 o = {pk2(a[0], a[1]), pk2(a[2], a[3]), pk2(a[4], a[5]), pk2(a[6], a[7])};
    *(u32x4*)(ws + A_OFF + ((size_t)(4 * g + tt) * 112 + 8 * j + w) * 1024 + lane * 16) = o;
  }
}

__global__ void __launch_bounds__(512, 2) k_dn(const uint16_t* __restrict__ wd, char* __restrict__ ws, uint16_t* __restrict__ out) {
  extern __shared__ __attribute__((aligned(16))) char smem_[];
  LAS char* smem = (LAS char*)smem_;
  int g, j; decode_block(blockIdx.x, g, j);
  if (j >= 14) return;
  const int th = j / 7, fc = j % 7;
  const int tid = threadIdx.x, lane = tid & 63, w = tid >> 6, hh = lane >> 5;
  const int tt = w & 1, kq = w >> 1;
  const int tile = 4 * g + 2 * th + tt;
  f32x16 acc0, acc1;
#pragma unroll
  for (int i = 0; i < 16; ++i) { acc0[i] = 0.f; acc1[i] = 0.f; }
  constexpr int WST = 128 + 64;
#pragma unroll 1
  for (int rd = 0; rd < 4; ++rd) {
    u32x4 af[7];
#pragma unroll
    for (int ks = 0; ks < 7; ++ks) af[ks] = *(const u32x4*)(ws + A_OFF + ((size_t)tile * 112 + 28 * kq + 7 * rd + ks) * 1024 + lane * 16);
    for (int i = tid; i < 448 * 8; i += 512) {
      int row = i >> 3, c = i & 7; int kq2 = row / 112, rr = row % 112;
      u32x4 v = *(const u32x4*)(wd + (size_t)(448 * kq2 + 112 * rd + rr) * D + 64 * fc + 8 * c);
      *(LAS u32x4*)(smem + WST * row + 16 * c) = v;
    }
    __syncthreads();
    const int blk = (lane >> 4) & 1;
    LAS char* wb = smem + WST * (112 * kq + 4 * hh + ((lane & 15) >> 2)) + 32 * blk + 8 * (lane & 3);
#pragma unroll
    for (int ks = 0; ks < 7; ++ks) {
      LAS char* p = wb + WST * 16 * ks;
      bf16x8 w0 = cat(trread(p), trread(p + 8 * WST));
      bf16x8 w1 = cat(trread(p + 64), trread(p + 8 * WST + 64));
      acc0 = mfma32(w0, asbf(af[ks]), acc0);
      acc1 = mfma32(w1, asbf(af[ks]), acc1);
    }
    __syncthreads();
  }
  LAS f32x4* red = (LAS f32x4*)smem;
#pragma unroll
  for (int i = 0; i < 4; ++i) {
    red[(((kq * 2 + tt) * 2 + 0) * 4 + i) * 64 + lane] = f32x4{acc0[4 * i], acc0[4 * i + 1], acc0[4 * i + 2], acc0[4 * i + 3]};
    red[(((kq * 2 + tt) * 2 + 1) * 4 + i) * 64 + lane] = f32x4{acc1[4 * i], acc1[4 * i + 1], acc1[4 * i + 2], acc1[4 * i + 3]};
  }
  __syncthreads();
  {
    const int t2 = w & 1, ft = (w >> 1) & 1, ip = w >> 2;
    const int tl = 4 * g + 2 * th + t2, tok = 32 * tl + (lane & 31);
#pragma unroll
    for (int ii = 0; ii < 2; ++ii) {
      const int i = 2 * ip + ii;
      f32x4 s = {0.f, 0.f, 0.f, 0.f};
#pragma unroll
      for (int k = 0; k < 4; ++k) s += red[(((k * 2 + t2) * 2 + ft) * 4 + i) * 64 + lane];
      const int fl = 8 * i + 4 * hh;
      const int qq = (fl & 15) >> 2, up = fl >> 4;
      const int ftg = 2 * fc + ft;
      u32x2 hv = *(const u32x2*)(ws + H_OFF + ((size_t)tl * 28 + 2 * ftg + (qq >> 1)) * 1024 + ((lane & 31) + 32 * (qq & 1)) * 16 + 8 * up);
      u32x2 o = {pk2(s[0] + bflo(hv[0]), s[1] + bfhi(hv[0])), pk2(s[2] + bflo(hv[1]), s[3] + bfhi(hv[1]))};
      *(u32x2*)(out + (size_t)tok * D + 32 * ftg + fl) = o;
    }
  }
}

extern "C" void kernel_launch(void* const* d_in, const int* in_sizes, int n_in, void* d_out, int out_size, void* d_ws, size_t ws_size, hipStream_t stream) {
  const uint16_t* x = (const uint16_t*)d_in[0];
  const uint16_t* wqkv = (const uint16_t*)d_in[1];
  const uint16_t* wo = (const uint16_t*)d_in[2];
  const uint16_t* wg = (const uint16_t*)d_in[4];
  const uint16_t* wu = (const uint16_t*)d_in[5];
  const uint16_t* wd = (const uint16_t*)d_in[6];
  char* ws = (char*)d_ws;
  hipFuncSetAttribute((const void*)k_qkv, hipFuncAttributeMaxDynamicSharedMemorySize, 90112);
  hipFuncSetAttribute((const void*)k_wo, hipFuncAttributeMaxDynamicSharedMemorySize, 73728);
  hipFuncSetAttribute((const void*)k_gu, hipFuncAttributeMaxDynamicSharedMemorySize, 131072);
  hipFuncSetAttribute((const void*)k_dn, hipFuncAttributeMaxDynamicSharedMemorySize, 90112);
  k_qkv<<<256, 512, 90112, stream>>>(x, wqkv, ws);
  k_attn<<<256, 512, 0, stream>>>(ws);
  k_wo<<<256, 512, 73728, stream>>>(x, wo, ws);
  k_gu<<<256, 512, 131072, stream>>>(wg, wu, ws);
  k_dn<<<256, 512, 90112, stream>>>(wd, ws, (uint16_t*)d_out);
}
```

```cpp
#include <hip/hip_runtime.h>
#include <stdint.h>
constexpr unsigned QKV_OFF = 1048576u, O_OFF = 7340032u, H_OFF = 9437184u, A_OFF = 11534336u, TS_OFF = 524288u;

extern "C" __global__ void __launch_bounds__(512, 2)
mega(const void* x, const void* wqkv, const void* wo, const void* wg, const void* wu, const void* wd, void* ws, void* out) {
  asm volatile(
    "s_load_dwordx2 s[4:5], s[0:1], 0x0\n"
    "s_load_dwordx2 s[8:9], s[0:1], 0x8\n"
    "s_getpc_b64 s[48:49]\n"
    ".Lpc0:\n"
    "s_and_b32 s50, s2, 0xf8\n"
    "v_lshrrev_b32_e32 v11, 5, v0\n"
    "v_lshl_add_u32 v11, s50, 1, v11\n"
    "v_lshlrev_b32_e32 v11, 7, v11\n"
    "v_min_u32_e32 v11, .Lmega_end-.Lpc0-4, v11\n"
    "global_load_dword v23, v11, s[48:49]\n"
    "s_load_dwordx2 s[12:13], s[0:1], 0x10\n"
    "s_load_dwordx2 s[16:17], s[0:1], 0x18\n"
    "s_load_dwordx2 s[20:21], s[0:1], 0x20\n"
    "s_load_dwordx2 s[24:25], s[0:1], 0x28\n"
    "s_load_dwordx2 s[28:29], s[0:1], 0x30\n"
    "s_load_dwordx2 s[32:33], s[0:1], 0x38\n"
    "s_mov_b32 s100, 4194304\n"
    "v_readfirstlane_b32 s3, v0\n"
    "v_and_b32_e32 v0, 63, v0\n"
    "s_lshr_b32 s3, s3, 6\n"
    "s_mov_b32 s6, 1835008\n"
    "s_mov_b32 s7, 0x20000\n"
    "s_mov_b32 s10, 1204224\n"
    "s_mov_b32 s11, 0x20000\n"
    "s_mov_b32 s14, 401408\n"
    "s_mov_b32 s15, 0x20000\n"
    "s_mov_b32 s18, 1605632\n"
    "s_mov_b32 s19, 0x20000\n"
    "s_mov_b32 s22, 1605632\n"
    "s_mov_b32 s23, 0x20000\n"
    "s_mov_b32 s26, 1605632\n"
    "s_mov_b32 s27, 0x20000\n"
    "s_mov_b32 s30, 18874368\n"
    "s_mov_b32 s31, 0x20000\n"
    "s_mov_b32 s34, 1835008\n"
    "s_mov_b32 s35, 0x20000\n"
    "s_lshr_b32 s51, s2, 3\n"
    "s_and_b32 s38, s2, 6\n"
    "s_lshl_b32 s38, s38, 1\n"
    "s_and_b32 s52, s51, 3\n"
    "s_add_u32 s38, s38, s52\n"
    "s_and_b32 s42, s2, 1\n"
    "s_mul_i32 s39, s42, 7\n"
    "s_lshr_b32 s43, s51, 2\n"
    "s_add_u32 s39, s39, s43\n"
    "s_and_b32 s41, s2, 7\n"
    "s_lshl_b32 s41, s41, 1\n"
    "s_and_b32 s52, s51, 1\n"
    "s_add_u32 s41, s41, s52\n"
    "s_lshr_b32 s40, s51, 1\n"
    "s_lshl_b32 s44, s3, 10\n"
    "s_mul_i32 s45, s3, 6144\n"
    "v_lshlrev_b32_e32 v1, 4, v0\n"
    "v_and_b32_e32 v2, 15, v0\n"
    "v_lshrrev_b32_e32 v3, 4, v0\n"
    "s_lshl_b32 s46, s39, 8\n"
    "s_cmp_ge_u32 s2, 224\n"
    "s_cbranch_scc1 .Lhelper0\n"
    "s_lshl_b32 s62, s38, 1\n"
    "s_add_u32 s62, s62, s39\n"
    "s_mul_i32 s52, s62, 37\n"
    "s_lshr_b32 s52, s52, 8\n"
    "s_mul_i32 s52, s52, 7\n"
    "s_sub_u32 s62, s62, s52\n"
    "v_and_b32_e32 v11, 14, v0\n"
    "v_mul_u32_u24_e32 v29, 896, v11\n"
    "v_lshl_add_u32 v29, v3, 5, v29\n"
    "v_and_b32_e32 v11, 16, v1\n"
    "v_add_u32_e32 v29, v29, v11\n"
    "s_lshr_b32 s52, s3, 1\n"
    "s_xor_b32 s52, s52, s3\n"
    "s_and_b32 s52, s52, 1\n"
    "s_lshl_b32 s52, s52, 5\n"
    "v_add_u32_e32 v11, 0, v0\n"
    "v_mul_u32_u24_e32 v17, 2731, v11\n"
    "v_lshrrev_b32_e32 v17, 15, v17\n"
    "v_mul_u32_u24_e32 v18, 12, v17\n"
    "v_sub_u32_e32 v11, v11, v18\n"
    "v_lshlrev_b32_e32 v11, 4, v11\n"
    "v_xor_b32_e32 v11, s52, v11\n"
    "v_mul_u32_u24_e32 v17, 2688, v17\n"
    "v_add_u32_e32 v26, v11, v17\n"
    "v_add_u32_e32 v11, 64, v0\n"
    "v_mul_u32_u24_e32 v17, 2731, v11\n"
    "v_lshrrev_b32_e32 v17, 15, v17\n"
    "v_mul_u32_u24_e32 v18, 12, v17\n"
    "v_sub_u32_e32 v11, v11, v18\n"
    "v_lshlrev_b32_e32 v11, 4, v11\n"
    "v_xor_b32_e32 v11, s52, v11\n"
    "v_mul_u32_u24_e32 v17, 2688, v17\n"
    "v_add_u32_e32 v27, v11, v17\n"
    "v_subrev_u32_e32 v27, 1024, v27\n"
    "v_add_u32_e32 v11, 128, v0\n"
    "v_mul_u32_u24_e32 v17, 2731, v11\n"
    "v_lshrrev_b32_e32 v17, 15, v17\n"
    "v_mul_u32_u24_e32 v18, 12, v17\n"
    "v_sub_u32_e32 v11, v11, v18\n"
    "v_lshlrev_b32_e32 v11, 4, v11\n"
    "v_xor_b32_e32 v11, s52, v11\n"
    "v_mul_u32_u24_e32 v17, 2688, v17\n"
    "v_add_u32_e32 v28, v11, v17\n"
    "v_subrev_u32_e32 v28, 2048, v28\n"
    "s_mul_i32 s53, s39, 192\n"
    "s_lshl_b32 s52, s62, 2\n"
    "s_add_u32 s52, s52, s3\n"
    "s_mul_i32 s48, s52, 43008\n"
    "s_add_u32 s48, s48, s53\n"
    "s_cmp_ge_u32 s48, 1204224\n"
    "s_cselect_b32 s52, 1204224, 0\n"
    "s_sub_u32 s48, s48, s52\n"
    "s_lshl_b32 s49, s38, 3\n"
    "s_add_u32 s49, s49, s3\n"
    "s_mul_i32 s50, s49, 14336\n"
    "s_add_u32 s63, s50, 896\n"
    "s_lshl_b32 s52, s62, 7\n"
    "s_add_u32 s50, s50, s52\n"
    "s_mul_i32 s51, s3, 3072\n"
    "s_add_u32 s51, s51, 77824\n"
    "s_mov_b32 m0, s51\n"
    "s_cmp_ge_u32 s3, 4\n"
    "s_waitcnt lgkmcnt(0)\n"
    "s_cbranch_scc1 .Lw0b\n"
    "buffer_load_dwordx4 v26, s[8:11], s48 offen offset:0  lds\n"
    "buffer_load_dwordx4 v27, s[8:11], s48 offen offset:1024  lds\n"
    "buffer_load_dwordx4 v28, s[8:11], s48 offen offset:2048  lds\n"
    "s_add_u32 s48, s48, 344064\n"
    "s_cmp_ge_u32 s48, 1204224\n"
    "s_cselect_b32 s55, 1204224, 0\n"
    "s_sub_u32 s48, s48, s55\n"
    "s_add_u32 s51, s51, 24576\n"
    ".Lw0b:\n"
    "buffer_load_dwordx4 v[30:33], v29, s[4:7], s50 offen offset:0\n"
    "buffer_load_dwordx4 v[34:37], v29, s[4:7], s50 offen offset:896\n"
    "s_add_u32 s50, s50, 128\n"
    "s_cmp_ge_u32 s50, s63\n"
    "s_cselect_b32 s55, 896, 0\n"
    "s_sub_u32 s50, s50, s55\n"
    "s_barrier\n"
    "s_add_u32 m0, s51, 0\n"
    "s_nop 0\n"
    "buffer_load_dwordx4 v26, s[8:11], s48 offen offset:0  lds\n"
    "s_add_u32 m0, s51, 0\n"
    "s_nop 0\n"
    "buffer_load_dwordx4 v27, s[8:11], s48 offen offset:1024  lds\n"
    "s_add_u32 m0, s51, 0\n"
    "s_nop 0\n"
    "buffer_load_dwordx4 v28, s[8:11], s48 offen offset:2048  lds\n"
    "s_add_u32 s48, s48, 344064\n"
    "s_cmp_ge_u32 s48, 1204224\n"
    "s_cselect_b32 s55, 1204224, 0\n"
    "s_sub_u32 s48, s48, s55\n"
    "buffer_load_dwordx4 v[38:41], v29, s[4:7], s50 offen offset:0\n"
    "buffer_load_dwordx4 v[42:45], v29, s[4:7], s50 offen offset:896\n"
    "s_add_u32 s50, s50, 128\n"
    "s_cmp_ge_u32 s50, s63\n"
    "s_cselect_b32 s55, 896, 0\n"
    "s_sub_u32 s50, s50, s55\n"
    "s_mul_i32 s54, s49, 43008\n"
    "s_mul_i32 s52, s39, 3072\n"
    "s_add_u32 s54, s54, s52\n"
    "s_add_u32 s54, s54, 1048576\n"
    "v_mov_b32_e32 v144, -1\n"
    "v_mov_b32_e32 v145, -1\n"
    "v_mov_b32_e32 v146, -1\n"
    "v_mov_b32_e32 v147, -1\n"
    "s_lshl_b32 s101, s38, 4\n"
    "s_add_u32 s101, s101, s39\n"
    "s_lshl_b32 s101, s101, 3\n"
    "s_add_u32 s101, s101, s3\n"
    "s_lshl_b32 s101, s101, 3\n"
    "s_add_u32 s101, s101, 4096\n"
    "s_lshl_b32 s71, s40, 3\n"
    "s_add_u32 s71, s71, s3\n"
    "s_lshl_b32 s71, s71, 4\n"
    "s_add_u32 s71, s71, s41\n"
    "s_lshl_b32 s70, s71, 10\n"
    "s_add_u32 s70, s70, 7340032\n"
    "s_lshl_b32 s71, s71, 3\n"
    "s_add_u32 s71, s71, 36864\n"
    "v_mov_b32_e32 v148, s101\n"
    "v_writelane_b32 v148, s71, 1\n"
    "s_load_dwordx2 s[36:37], s[28:29], 0x0\n"
    "s_mul_i32 s68, s41, 7\n"
    "s_lshr_b32 s68, s68, 1\n"
    "s_lshl_b32 s71, s40, 3\n"
    "s_add_u32 s69, s71, s3\n"
    "s_mul_i32 s47, s3, 3\n"
    "s_add_u32 s71, s71, s47\n"
    "s_sub_u32 s71, s71, 16\n"
    "v_min_u32_e32 v11, 13, v0\n"
    "v_lshrrev_b32_e32 v16, 1, v11\n"
    "v_add_u32_e32 v16, 2, v16\n"
    "v_mul_u32_u24_e32 v17, 11, v16\n"
    "v_lshrrev_b32_e32 v17, 5, v17\n"
    "v_mul_u32_u24_e32 v18, 3, v17\n"
    "v_sub_u32_e32 v16, v16, v18\n"
    "v_add_u32_e32 v16, s71, v16\n"
    "v_max_i32_e32 v16, 0, v16\n"
    "v_writelane_b32 v16, s69, 0\n"
    "v_writelane_b32 v16, s69, 1\n"
    "v_and_b32_e32 v11, 1, v11\n"
    "v_mul_u32_u24_e32 v11, 3, v11\n"
    "v_mad_u32_u24 v11, v17, 56, v11\n"
    "v_add_u32_e32 v11, s68, v11\n"
    "v_mul_u32_u24_e32 v11, 171, v11\n"
    "v_lshrrev_b32_e32 v11, 11, v11\n"
    "v_lshrrev_b32_e32 v17, 3, v16\n"
    "v_lshl_add_u32 v11, v17, 4, v11\n"
    "v_lshlrev_b32_e32 v11, 6, v11\n"
    "v_and_b32_e32 v16, 7, v16\n"
    "v_lshl_add_u32 v19, v16, 3, v11\n"
    "v_add_u32_e32 v19, 4096, v19\n"
    "v_mov_b32_e32 v12, 0x3f803f80\n"
    "v_mov_b32_e32 v13, 0x3f803f80\n"
    "v_mov_b32_e32 v14, 0x3f803f80\n"
    "v_mov_b32_e32 v15, 0x3f803f80\n"
    "v_mov_b32_e32 v24, 0\n"
    "v_mov_b32_e32 v25, 0\n"
    "s_lshl_b32 s52, s3, 2\n"
    "v_add_u32_e32 v11, s52, v3\n"
    "v_mul_u32_u24_e32 v6, 3584, v11\n"
    "v_lshrrev_b32_e32 v11, 1, v2\n"
    "v_xor_b32_e32 v11, v11, v3\n"
    "v_lshl_add_u32 v6, v11, 5, v6\n"
    "v_and_b32_e32 v11, 16, v1\n"
    "v_add_u32_e32 v6, v6, v11\n"
    "v_xor_b32_e32 v7, 0x80, v6\n"
    "v_lshrrev_b32_e32 v11, 2, v2\n"
    "v_and_b32_e32 v17, 1, v3\n"
    "v_lshl_add_u32 v18, v17, 2, v11\n"
    "v_xor_b32_e32 v18, s3, v18\n"
    "v_lshlrev_b32_e32 v8, 5, v18\n"
    "v_lshl_add_u32 v8, v17, 13, v8\n"
    "v_lshl_add_u32 v8, v11, 8, v8\n"
    "v_lshrrev_b32_e32 v18, 5, v0\n"
    "v_lshl_add_u32 v8, v18, 10, v8\n"
    "v_and_b32_e32 v18, 3, v0\n"
    "v_lshl_add_u32 v8, v18, 3, v8\n"
    "v_add_u32_e32 v9, 0x10000, v8\n"
    "v_add_u32_e32 v10, 0x20000, v8\n"
    "v_lshl_add_u32 v16, v3, 4, v11\n"
    "v_mul_u32_u24_e32 v16, 192, v16\n"
    "v_lshl_add_u32 v16, v18, 3, v16\n"
    "v_add_u32_e32 v16, 77824, v16\n"
    "v_lshrrev_b32_e32 v11, 1, v3\n"
    "v_xor_b32_e32 v11, v11, v17\n"
    "s_add_u32 s56, s3, 0\n"
    "s_cmp_ge_u32 s56, 6\n"
    "s_cselect_b32 s52, 6, 0\n"
    "s_sub_u32 s56, s56, s52\n"
    "v_xor_b32_e32 v92, s56, v11\n"
    "v_lshl_add_u32 v86, v92, 5, v16\n"
    "v_add_u32_e32 v92, 49152, v86\n"
    "s_add_u32 s57, s3, 1\n"
    "s_cmp_ge_u32 s57, 6\n"
    "s_cselect_b32 s52, 6, 0\n"
    "s_sub_u32 s57, s57, s52\n"
    "v_xor_b32_e32 v93, s57, v11\n"
    "v_lshl_add_u32 v87, v93, 5, v16\n"
    "v_add_u32_e32 v93, 49152, v87\n"
    "s_add_u32 s58, s3, 2\n"
    "s_cmp_ge_u32 s58, 6\n"
    "s_cselect_b32 s52, 6, 0\n"
    "s_sub_u32 s58, s58, s52\n"
    "v_xor_b32_e32 v94, s58, v11\n"
    "v_lshl_add_u32 v88, v94, 5, v16\n"
    "v_add_u32_e32 v94, 49152, v88\n"
    "s_add_u32 s59, s3, 3\n"
    "s_cmp_ge_u32 s59, 6\n"
    "s_cselect_b32 s52, 6, 0\n"
    "s_sub_u32 s59, s59, s52\n"
    "v_xor_b32_e32 v95, s59, v11\n"
    "v_lshl_add_u32 v89, v95, 5, v16\n"
    "v_add_u32_e32 v95, 49152, v89\n"
    "s_add_u32 s60, s3, 4\n"
    "s_cmp_ge_u32 s60, 6\n"
    "s_cselect_b32 s52, 6, 0\n"
    "s_sub_u32 s60, s60, s52\n"
    "v_xor_b32_e32 v96, s60, v11\n"
    "v_lshl_add_u32 v90, v96, 5, v16\n"
    "v_add_u32_e32 v96, 49152, v90\n"
    "s_add_u32 s61, s3, 5\n"
    "s_cmp_ge_u32 s61, 6\n"
    "s_cselect_b32 s52, 6, 0\n"
    "s_sub_u32 s61, s61, s52\n"
    "s_cmp_ge_u32 s61, 6\n"
    "s_cselect_b32 s52, 6, 0\n"
    "s_sub_u32 s61, s61, s52\n"
    "v_xor_b32_e32 v97, s61, v11\n"
    "v_lshl_add_u32 v91, v97, 5, v16\n"
    "v_add_u32_e32 v97, 49152, v91\n"
    "s_mov_b32 s64, 0x55555555\n"
    "s_mov_b32 s65, 0x55555555\n"
    "s_mov_b32 s66, 0xaaaaaaaa\n"
    "s_mov_b32 s67, 0xaaaaaaaa\n"
    "s_waitcnt lgkmcnt(0)\n"
    "s_add_u32 s36, s36, 0x7f4a7c15\n"
    "s_addc_u32 s37, s37, 0x9e3779b9\n"
    "v_mov_b32_e32 v4, s36\n"
    "v_mov_b32_e32 v5, s37\n"
    "s_mov_b64 vcc, s[64:65]\n"
    "s_waitcnt vmcnt(5)\n"
    "v_cndmask_b32_dpp v160, v34, v30, vcc quad_perm:[1,0,3,2] row_mask:0xf bank_mask:0xf\n"
    "v_cndmask_b32_dpp v161, v35, v31, vcc quad_perm:[1,0,3,2] row_mask:0xf bank_mask:0xf\n"
    "v_cndmask_b32_dpp v162, v36, v32, vcc quad_perm:[1,0,3,2] row_mask:0xf bank_mask:0xf\n"
    "v_cndmask_b32_dpp v163, v37, v33, vcc quad_perm:[1,0,3,2] row_mask:0xf bank_mask:0xf\n"
    "s_mov_b64 vcc, s[66:67]\n"
    "s_nop 0\n"
    "v_cndmask_b32_dpp v34, v30, v34, vcc quad_perm:[1,0,3,2] row_mask:0xf bank_mask:0xf\n"
    "v_cndmask_b32_dpp v35, v31, v35, vcc quad_perm:[1,0,3,2] row_mask:0xf bank_mask:0xf\n"
    "v_cndmask_b32_dpp v36, v32, v36, vcc quad_perm:[1,0,3,2] row_mask:0xf bank_mask:0xf\n"
    "v_cndmask_b32_dpp v37, v33, v37, vcc quad_perm:[1,0,3,2] row_mask:0xf bank_mask:0xf\n"
    "s_barrier\n"
    "ds_read_b64_tr_b16 v[192:193], v86 offset:0\n"
    "ds_read_b64_tr_b16 v[194:195], v86 offset:768\n"
    "ds_read_b64_tr_b16 v[196:197], v87 offset:0\n"
    "ds_read_b64_tr_b16 v[198:199], v87 offset:768\n"
    "ds_read_b64_tr_b16 v[200:201], v88 offset:0\n"
    "ds_read_b64_tr_b16 v[202:203], v88 offset:768\n"
    "ds_read_b64_tr_b16 v[204:205], v89 offset:0\n"
    "ds_read_b64_tr_b16 v[206:207], v89 offset:768\n"
    "ds_read_b64_tr_b16 v[208:209], v90 offset:0\n"
    "ds_read_b64_tr_b16 v[210:211], v90 offset:768\n"
    "ds_read_b64_tr_b16 v[212:213], v91 offset:0\n"
    "ds_read_b64_tr_b16 v[214:215], v91 offset:768\n"
    "ds_read_b64_tr_b16 v[216:217], v86 offset:1536\n"
    "ds_read_b64_tr_b16 v[218:219], v86 offset:2304\n"
    "v_mfma_f32_16x16x32_bf16 v[172:175], v[12:15], v[160:163], 0\n"
    "s_add_u32 m0, s51, 24576\n"
    "s_nop 0\n"
    "buffer_load_dwordx4 v26, s[8:11], s48 offen offset:0  lds\n"
    "v_mfma_f32_16x16x32_bf16 v[164:167], v[160:163], v[160:163], 0\n"
    "s_waitcnt lgkmcnt(12)\n"
    "v_mfma_f32_16x16x32_bf16 v[168:171], v[192:195], v[12:15], 0\n"
    "s_add_u32 m0, s51, 24576\n"
    "s_nop 0\n"
    "buffer_load_dwordx4 v27, s[8:11], s48 offen offset:1024  lds\n"
    "v_mfma_f32_16x16x32_bf16 v[100:103], v[192:195], v[160:163], 0\n"
    "ds_read_b64_tr_b16 v[220:221], v87 offset:1536\n"
    "ds_read_b64_tr_b16 v[222:223], v87 offset:2304\n"
    "s_waitcnt lgkmcnt(12)\n"
    "v_mfma_f32_16x16x32_bf16 v[104:107], v[196:199], v[160:163], 0\n"
    "ds_read_b64_tr_b16 v[224:225], v88 offset:1536\n"
    "ds_read_b64_tr_b16 v[226:227], v88 offset:2304\n"
    "s_add_u32 m0, s51, 24576\n"
    "s_nop 0\n"
    "buffer_load_dwordx4 v28, s[8:11], s48 offen offset:2048  lds\n"
    "s_add_u32 s48, s48, 344064\n"
    "s_cmp_ge_u32 s48, 1204224\n"
    "s_cselect_b32 s55, 1204224, 0\n"
    "s_sub_u32 s48, s48, s55\n"
    "s_waitcnt lgkmcnt(12)\n"
    "v_mfma_f32_16x16x32_bf16 v[108:111], v[200:203], v[160:163], 0\n"
    "ds_read_b64_tr_b16 v[228:229], v89 offset:1536\n"
    "ds_read_b64_tr_b16 v[230:231], v89 offset:2304\n"
    "s_waitcnt lgkmcnt(12)\n"
    "v_mfma_f32_16x16x32_bf16 v[112:115], v[204:207], v[160:163], 0\n"
    "ds_read_b64_tr_b16 v[232:233], v90 offset:1536\n"
    "ds_read_b64_tr_b16 v[234:235], v90 offset:2304\n"
    "buffer_load_dwordx4 v[46:49], v29, s[4:7], s50 offen offset:0\n"
    "s_waitcnt lgkmcnt(12)\n"
    "v_mfma_f32_16x16x32_bf16 v[116:119], v[208:211], v[160:163], 0\n"
    "ds_read_b64_tr_b16 v[236:237], v91 offset:1536\n"
    "ds_read_b64_tr_b16 v[238:239], v91 offset:2304\n"
    "s_waitcnt lgkmcnt(12)\n"
    "v_mfma_f32_16x16x32_bf16 v[120:123], v[212:215], v[160:163], 0\n"
    "s_waitcnt vmcnt(6)\n"
    "s_barrier\n"
    "ds_read_b64_tr_b16 v[240:241], v86 offset:12288\n"
    "ds_read_b64_tr_b16 v[242:243], v86 offset:13056\n"
    "buffer_load_dwordx4 v[50:53], v29, s[4:7], s50 offen offset:896\n"
    "s_add_u32 s50, s50, 128\n"
    "s_cmp_ge_u32 s50, s63\n"
    "s_cselect_b32 s55, 896, 0\n"
    "s_sub_u32 s50, s50, s55\n"
    "v_mfma_f32_16x16x32_bf16 v[172:175], v[12:15], v[34:37], v[172:175]\n"
    "v_mfma_f32_16x16x32_bf16 v[164:167], v[34:37], v[34:37], v[164:167]\n"
    "buffer_store_dwordx4 v[144:147], v1, s[28:31], s54 offen offset:0 sc1\n"
    "s_waitcnt lgkmcnt(12)\n"
    "v_mfma_f32_16x16x32_bf16 v[168:171], v[216:219], v[12:15], v[168:171]\n"
    "v_mfma_f32_16x16x32_bf16 v[100:103], v[216:219], v[34:37], v[100:103]\n"
    "ds_read_b64_tr_b16 v[244:245], v87 offset:12288\n"
    "ds_read_b64_tr_b16 v[246:247], v87 offset:13056\n"
    "s_waitcnt lgkmcnt(12)\n"
    "v_mfma_f32_16x16x32_bf16 v[104:107], v[220:223], v[34:37], v[104:107]\n"
    "ds_read_b64_tr_b16 v[248:249], v88 offset:12288\n"
    "ds_read_b64_tr_b16 v[250:251], v88 offset:13056\n"
    "s_mov_b64 vcc, s[64:65]\n"
    "s_waitcnt vmcnt(6)\n"
    "v_cndmask_b32_dpp v30, v42, v38, vcc quad_perm:[1,0,3,2] row_mask:0xf bank_mask:0xf\n"
    "v_cndmask_b32_dpp v31, v43, v39, vcc quad_perm:[1,0,3,2] row_mask:0xf bank_mask:0xf\n"
    "v_cndmask_b32_dpp v32, v44, v40, vcc quad_perm:[1,0,3,2] row_mask:0xf bank_mask:0xf\n"
    "v_cndmask_b32_dpp v33, v45, v41, vcc quad_perm:[1,0,3,2] row_mask:0xf bank_mask:0xf\n"
    "s_waitcnt lgkmcnt(12)\n"
    "v_mfma_f32_16x16x32_bf16 v[108:111], v[224:227], v[34:37], v[108:111]\n"
    "ds_read_b64_tr_b16 v[252:253], v89 offset:12288\n"
    "ds_read_b64_tr_b16 v[254:255], v89 offset:13056\n"
    "s_waitcnt lgkmcnt(12)\n"
    "v_mfma_f32_16x16x32_bf16 v[112:115], v[228:231], v[34:37], v[112:115]\n"
    "ds_read_b64_tr_b16 v[192:193], v90 offset:12288\n"
    "ds_read_b64_tr_b16 v[194:195], v90 offset:13056\n"
    "s_mov_b64 vcc, s[66:67]\n"
    "s_nop 0\n"
    "v_cndmask_b32_dpp v42, v38, v42, vcc quad_perm:[1,0,3,2] row_mask:0xf bank_mask:0xf\n"
    "v_cndmask_b32_dpp v43, v39, v43, vcc quad_perm:[1,0,3,2] row_mask:0xf bank_mask:0xf\n"
    "v_cndmask_b32_dpp v44, v40, v44, vcc quad_perm:[1,0,3,2] row_mask:0xf bank_mask:0xf\n"
    "v_cndmask_b32_dpp v45, v41, v45, vcc quad_perm:[1,0,3,2] row_mask:0xf bank_mask:0xf\n"
    "buffer_store_dwordx4 v[144:147], v1, s[28:31], s54 offen offset:1024 sc1\n"
    "s_waitcnt lgkmcnt(12)\n"
    "v_mfma_f32_16x16x32_bf16 v[116:119], v[232:235], v[34:37], v[116:119]\n"
    "ds_read_b64_tr_b16 v[196:197], v91 offset:12288\n"
    "ds_read_b64_tr_b16 v[198:199], v91 offset:13056\n"
    "s_waitcnt lgkmcnt(12)\n"
    "v_mfma_f32_16x16x32_bf16 v[120:123], v[236:239], v[34:37], v[120:123]\n"
    "ds_read_b64_tr_b16 v[200:201], v86 offset:13824\n"
    "ds_read_b64_tr_b16 v[202:203], v86 offset:14592\n"
    "v_mfma_f32_16x16x32_bf16 v[172:175], v[12:15], v[30:33], v[172:175]\n"
    "buffer_load_dwordx4 v[54:57], v29, s[4:7], s50 offen offset:0\n"
    "v_mfma_f32_16x16x32_bf16 v[164:167], v[30:33], v[30:33], v[164:167]\n"
    "s_waitcnt lgkmcnt(12)\n"
    "v_mfma_f32_16x16x32_bf16 v[168:171], v[240:243], v[12:15], v[168:171]\n"
    "buffer_load_dwordx4 v[58:61], v29, s[4:7], s50 offen offset:896\n"
    "s_add_u32 s50, s50, 128\n"
    "s_cmp_ge_u32 s50, s63\n"
    "s_cselect_b32 s55, 896, 0\n"
    "s_sub_u32 s50, s50, s55\n"
    "buffer_store_dwordx4 v[144:147], v1, s[28:31], s54 offen offset:2048 sc1\n"
    "v_mfma_f32_16x16x32_bf16 v[100:103], v[240:243], v[30:33], v[100:103]\n"
    "ds_read_b64_tr_b16 v[204:205], v87 offset:13824\n"
    "ds_read_b64_tr_b16 v[206:207], v87 offset:14592\n"
    "s_waitcnt lgkmcnt(12)\n"
    "v_mfma_f32_16x16x32_bf16 v[104:107], v[244:247], v[30:33], v[104:107]\n"
    "ds_read_b64_tr_b16 v[208:209], v88 offset:13824\n"
    "ds_read_b64_tr_b16 v[210:211], v88 offset:14592\n"
    "s_add_u32 m0, s51, 49152\n"
    "s_nop 0\n"
    "buffer_load_dwordx4 v26, s[8:11], s48 offen offset:0  lds\n"
    "s_waitcnt lgkmcnt(12)\n"
    "v_mfma_f32_16x16x32_bf16 v[108:111], v[248:251], v[30:33], v[108:111]\n"
    "ds_read_b64_tr_b16 v[212:213], v89 offset:13824\n"
    "ds_read_b64_tr_b16 v[214:215], v89 offset:14592\n"
    "s_waitcnt lgkmcnt(12)\n"
    "v_mfma_f32_16x16x32_bf16 v[112:115], v[252:255], v[30:33], v[112:115]\n"
    "ds_read_b64_tr_b16 v[216:217], v90 offset:13824\n"
    "ds_read_b64_tr_b16 v[218:219], v90 offset:14592\n"
    "s_add_u32 m0, s51, 49152\n"
    "s_nop 0\n"
    "buffer_load_dwordx4 v27, s[8:11], s48 offen offset:1024  lds\n"
    "s_waitcnt lgkmcnt(12)\n"
    "v_mfma_f32_16x16x32_bf16 v[116:119], v[192:195], v[30:33], v[116:119]\n"
    "ds_read_b64_tr_b16 v[220:221], v91 offset:13824\n"
    "ds_read_b64_tr_b16 v[222:223], v91 offset:14592\n"
    "buffer_store_dwordx4 v[144:147], v1, s[28:31], s70 offen sc1\n"
    "s_waitcnt lgkmcnt(12)\n"
    "v_mfma_f32_16x16x32_bf16 v[120:123], v[196:199], v[30:33], v[120:123]\n"
    "ds_read_b64_tr_b16 v[224:225], v86 offset:24576\n"
    "ds_read_b64_tr_b16 v[226:227], v86 offset:25344\n"
    "s_add_u32 m0, s51, 49152\n"
    "s_nop 0\n"
    "buffer_load_dwordx4 v28, s[8:11], s48 offen offset:2048  lds\n"
    "s_add_u32 s48, s48, 344064\n"
    "s_cmp_ge_u32 s48, 1204224\n"
    "s_cselect_b32 s55, 1204224, 0\n"
    "s_sub_u32 s48, s48, s55\n"
    "v_mfma_f32_16x16x32_bf16 v[172:175], v[12:15], v[42:45], v[172:175]\n"
    "v_mfma_f32_16x16x32_bf16 v[164:167], v[42:45], v[42:45], v[164:167]\n"
    "s_waitcnt lgkmcnt(12)\n"
    "v_mfma_f32_16x16x32_bf16 v[168:171], v[200:203], v[12:15], v[168:171]\n"
    "v_mfma_f32_16x16x32_bf16 v[100:103], v[200:203], v[42:45], v[100:103]\n"
    "ds_read_b64_tr_b16 v[228:229], v87 offset:24576\n"
    "ds_read_b64_tr_b16 v[230:231], v87 offset:25344\n"
    "s_waitcnt lgkmcnt(12)\n"
    "v_mfma_f32_16x16x32_bf16 v[104:107], v[204:207], v[42:45], v[104:107]\n"
    "ds_read_b64_tr_b16 v[232:233], v88 offset:24576\n"
    "ds_read_b64_tr_b16 v[234:235], v88 offset:25344\n"
    "s_mov_b64 vcc, s[64:65]\n"
    "s_waitcnt vmcnt(9)\n"
    "v_cndmask_b32_dpp v38, v50, v46, vcc quad_perm:[1,0,3,2] row_mask:0xf bank_mask:0xf\n"
    "v_cndmask_b32_dpp v39, v51, v47, vcc quad_perm:[1,0,3,2] row_mask:0xf bank_mask:0xf\n"
    "v_cndmask_b32_dpp v40, v52, v48, vcc quad_perm:[1,0,3,2] row_mask:0xf bank_mask:0xf\n"
    "v_cndmask_b32_dpp v41, v53, v49, vcc quad_perm:[1,0,3,2] row_mask:0xf bank_mask:0xf\n"
    "s_waitcnt lgkmcnt(12)\n"
    "v_mfma_f32_16x16x32_bf16 v[108:111], v[208:211], v[42:45], v[108:111]\n"
    "ds_read_b64_tr_b16 v[236:237], v89 offset:24576\n"
    "ds_read_b64_tr_b16 v[238:239], v89 offset:25344\n"
    "buffer_load_dwordx4 v[62:65], v29, s[4:7], s50 offen offset:0\n"
    "s_waitcnt lgkmcnt(12)\n"
    "v_mfma_f32_16x16x32_bf16 v[112:115], v[212:215], v[42:45], v[112:115]\n"
    "ds_read_b64_tr_b16 v[240:241], v90 offset:24576\n"
    "ds_read_b64_tr_b16 v[242:243], v90 offset:25344\n"
    "s_mov_b64 vcc, s[66:67]\n"
    "s_nop 0\n"
    "v_cndmask_b32_dpp v50, v46, v50, vcc quad_perm:[1,0,3,2] row_mask:0xf bank_mask:0xf\n"
    "v_cndmask_b32_dpp v51, v47, v51, vcc quad_perm:[1,0,3,2] row_mask:0xf bank_mask:0xf\n"
    "v_cndmask_b32_dpp v52, v48, v52, vcc quad_perm:[1,0,3,2] row_mask:0xf bank_mask:0xf\n"
    "v_cndmask_b32_dpp v53, v49, v53, vcc quad_perm:[1,0,3,2] row_mask:0xf bank_mask:0xf\n"
    "s_waitcnt lgkmcnt(12)\n"
    "v_mfma_f32_16x16x32_bf16 v[116:119], v[216:219], v[42:45], v[116:119]\n"
    "ds_read_b64_tr_b16 v[244:245], v91 offset:24576\n"
    "ds_read_b64_tr_b16 v[246:247], v91 offset:25344\n"
    "buffer_load_dwordx4 v[66:69], v29, s[4:7], s50 offen offset:896\n"
    "s_add_u32 s50, s50, 128\n"
    "s_cmp_ge_u32 s50, s63\n"
    "s_cselect_b32 s55, 896, 0\n"
    "s_sub_u32 s50, s50, s55\n"
    "s_waitcnt lgkmcnt(12)\n"
    "v_mfma_f32_16x16x32_bf16 v[120:123], v[220:223], v[42:45], v[120:123]\n"
    "ds_read_b64_tr_b16 v[248:249], v86 offset:26112\n"
    "ds_read_b64_tr_b16 v[250:251], v86 offset:26880\n"
    "v_mfma_f32_16x16x32_bf16 v[172:175], v[12:15], v[38:41], v[172:175]\n"
    "v_mfma_f32_16x16x32_bf16 v[164:167], v[38:41], v[38:41], v[164:167]\n"
    "s_waitcnt lgkmcnt(12)\n"
    "v_mfma_f32_16x16x32_bf16 v[168:171], v[224:227], v[12:15], v[168:171]\n"
    "v_mfma_f32_16x16x32_bf16 v[100:103], v[224:227], v[38:41], v[100:103]\n"
    "ds_read_b64_tr_b16 v[252:253], v87 offset:26112\n"
    "ds_read_b64_tr_b16 v[254:255], v87 offset:26880\n"
    "s_waitcnt lgkmcnt(12)\n"
    "v_mfma_f32_16x16x32_bf16 v[104:107], v[228:231], v[38:41], v[104:107]\n"
    "ds_read_b64_tr_b16 v[192:193], v88 offset:26112\n"
    "ds_read_b64_tr_b16 v[194:195], v88 offset:26880\n"
    "s_waitcnt lgkmcnt(12)\n"
    "v_mfma_f32_16x16x32_bf16 v[108:111], v[232:235], v[38:41], v[108:111]\n"
    "ds_read_b64_tr_b16 v[196:197], v89 offset:26112\n"
    "ds_read_b64_tr_b16 v[198:199], v89 offset:26880\n"
    "s_waitcnt lgkmcnt(12)\n"
    "v_mfma_f32_16x16x32_bf16 v[112:115], v[236:239], v[38:41], v[112:115]\n"
    "ds_read_b64_tr_b16 v[200:201], v90 offset:26112\n"
    "ds_read_b64_tr_b16 v[202:203], v90 offset:26880\n"
    "s_waitcnt lgkmcnt(12)\n"
    "v_mfma_f32_16x16x32_bf16 v[116:119], v[240:243], v[38:41], v[116:119]\n"
    "ds_read_b64_tr_b16 v[204:205], v91 offset:26112\n"
    "ds_read_b64_tr_b16 v[206:207], v91 offset:26880\n"
    "s_waitcnt lgkmcnt(12)\n"
    "v_mfma_f32_16x16x32_bf16 v[120:123], v[244:247], v[38:41], v[120:123]\n"
    "s_barrier\n"
    "ds_read_b64_tr_b16 v[208:209], v86 offset:36864\n"
    "ds_read_b64_tr_b16 v[210:211], v86 offset:37632\n"
    "buffer_load_dwordx4 v[70:73], v29, s[4:7], s50 offen offset:0\n"
    "v_mfma_f32_16x16x32_bf16 v[172:175], v[12:15], v[50:53], v[172:175]\n"
    "v_mfma_f32_16x16x32_bf16 v[164:167], v[50:53], v[50:53], v[164:167]\n"
    "buffer_load_dwordx4 v[74:77], v29, s[4:7], s50 offen offset:896\n"
    "s_add_u32 s50, s50, 128\n"
    "s_cmp_ge_u32 s50, s63\n"
    "s_cselect_b32 s55, 896, 0\n"
    "s_sub_u32 s50, s50, s55\n"
    "s_waitcnt lgkmcnt(12)\n"
    "v_mfma_f32_16x16x32_bf16 v[168:171], v[248:251], v[12:15], v[168:171]\n"
    "v_mfma_f32_16x16x32_bf16 v[100:103], v[248:251], v[50:53], v[100:103]\n"
    "ds_read_b64_tr_b16 v[212:213], v87 offset:36864\n"
    "ds_read_b64_tr_b16 v[214:215], v87 offset:37632\n"
    "s_waitcnt lgkmcnt(12)\n"
    "v_mfma_f32_16x16x32_bf16 v[104:107], v[252:255], v[50:53], v[104:107]\n"
    "ds_read_b64_tr_b16 v[216:217], v88 offset:36864\n"
    "ds_read_b64_tr_b16 v[218:219], v88 offset:37632\n"
    "s_mov_b64 vcc, s[64:65]\n"
    "s_waitcnt vmcnt(9)\n"
    "v_cndmask_b32_dpp v46, v58, v54, vcc quad_perm:[1,0,3,2] row_mask:0xf bank_mask:0xf\n"
    "v_cndmask_b32_dpp v47, v59, v55, vcc quad_perm:[1,0,3,2] row_mask:0xf bank_mask:0xf\n"
    "v_cndmask_b32_dpp v48, v60, v56, vcc quad_perm:[1,0,3,2] row_mask:0xf bank_mask:0xf\n"
    "v_cndmask_b32_dpp v49, v61, v57, vcc quad_perm:[1,0,3,2] row_mask:0xf bank_mask:0xf\n"
    "s_waitcnt lgkmcnt(12)\n"
    "v_mfma_f32_16x16x32_bf16 v[108:111], v[192:195], v[50:53], v[108:111]\n"
    "ds_read_b64_tr_b16 v[220:221], v89 offset:36864\n"
    "ds_read_b64_tr_b16 v[222:223], v89 offset:37632\n"
    "s_waitcnt lgkmcnt(12)\n"
    "v_mfma_f32_16x16x32_bf16 v[112:115], v[196:199], v[50:53], v[112:115]\n"
    "ds_read_b64_tr_b16 v[224:225], v90 offset:36864\n"
    "ds_read_b64_tr_b16 v[226:227], v90 offset:37632\n"
    "s_mov_b64 vcc, s[66:67]\n"
    "s_nop 0\n"
    "v_cndmask_b32_dpp v58, v54, v58, vcc quad_perm:[1,0,3,2] row_mask:0xf bank_mask:0xf\n"
    "v_cndmask_b32_dpp v59, v55, v59, vcc quad_perm:[1,0,3,2] row_mask:0xf bank_mask:0xf\n"
    "v_cndmask_b32_dpp v60, v56, v60, vcc quad_perm:[1,0,3,2] row_mask:0xf bank_mask:0xf\n"
    "v_cndmask_b32_dpp v61, v57, v61, vcc quad_perm:[1,0,3,2] row_mask:0xf bank_mask:0xf\n"
    "s_waitcnt lgkmcnt(12)\n"
    "v_mfma_f32_16x16x32_bf16 v[116:119], v[200:203], v[50:53], v[116:119]\n"
    "ds_read_b64_tr_b16 v[228:229], v91 offset:36864\n"
    "ds_read_b64_tr_b16 v[230:231], v91 offset:37632\n"
    "s_waitcnt lgkmcnt(12)\n"
    "v_mfma_f32_16x16x32_bf16 v[120:123], v[204:207], v[50:53], v[120:123]\n"
    "ds_read_b64_tr_b16 v[232:233], v86 offset:38400\n"
    "ds_read_b64_tr_b16 v[234:235], v86 offset:39168\n"
    "v_mfma_f32_16x16x32_bf16 v[172:175], v[12:15], v[46:49], v[172:175]\n"
    "v_mfma_f32_16x16x32_bf16 v[164:167], v[46:49], v[46:49], v[164:167]\n"
    "s_waitcnt lgkmcnt(12)\n"
    "v_mfma_f32_16x16x32_bf16 v[168:171], v[208:211], v[12:15], v[168:171]\n"
    "buffer_load_dwordx4 v[78:81], v29, s[4:7], s50 offen offset:0\n"
    "v_mfma_f32_16x16x32_bf16 v[100:103], v[208:211], v[46:49], v[100:103]\n"
    "ds_read_b64_tr_b16 v[236:237], v87 offset:38400\n"
    "ds_read_b64_tr_b16 v[238:239], v87 offset:39168\n"
    "s_waitcnt lgkmcnt(12)\n"
    "v_mfma_f32_16x16x32_bf16 v[104:107], v[212:215], v[46:49], v[104:107]\n"
    "ds_read_b64_tr_b16 v[240:241], v88 offset:38400\n"
    "ds_read_b64_tr_b16 v[242:243], v88 offset:39168\n"
    "buffer_load_dwordx4 v[82:85], v29, s[4:7], s50 offen offset:896\n"
    "s_waitcnt lgkmcnt(12)\n"
    "v_mfma_f32_16x16x32_bf16 v[108:111], v[216:219], v[46:49], v[108:111]\n"
    "ds_read_b64_tr_b16 v[244:245], v89 offset:38400\n"
    "ds_read_b64_tr_b16 v[246:247], v89 offset:39168\n"
    "s_waitcnt lgkmcnt(12)\n"
    "v_mfma_f32_16x16x32_bf16 v[112:115], v[220:223], v[46:49], v[112:115]\n"
    "ds_read_b64_tr_b16 v[248:249], v90 offset:38400\n"
    "ds_read_b64_tr_b16 v[250:251], v90 offset:39168\n"
    "s_waitcnt lgkmcnt(12)\n"
    "v_mfma_f32_16x16x32_bf16 v[116:119], v[224:227], v[46:49], v[116:119]\n"
    "ds_read_b64_tr_b16 v[252:253], v91 offset:38400\n"
    "ds_read_b64_tr_b16 v[254:255], v91 offset:39168\n"
    "s_waitcnt lgkmcnt(12)\n"
    "v_mfma_f32_16x16x32_bf16 v[120:123], v[228:231], v[46:49], v[120:123]\n"
    "ds_read_b64_tr_b16 v[192:193], v92 offset:0\n"
    "ds_read_b64_tr_b16 v[194:195], v92 offset:768\n"
    "v_mfma_f32_16x16x32_bf16 v[172:175], v[12:15], v[58:61], v[172:175]\n"
    "v_mfma_f32_16x16x32_bf16 v[164:167], v[58:61], v[58:61], v[164:167]\n"
    "s_waitcnt lgkmcnt(12)\n"
    "v_mfma_f32_16x16x32_bf16 v[168:171], v[232:235], v[12:15], v[168:171]\n"
    "v_mfma_f32_16x16x32_bf16 v[100:103], v[232:235], v[58:61], v[100:103]\n"
    "ds_read_b64_tr_b16 v[196:197], v92 offset:1536\n"
    "ds_read_b64_tr_b16 v[198:199], v92 offset:2304\n"
    "s_waitcnt vmcnt(7)\n"
    "s_mov_b64 exec, 3\n"
    "buffer_store_dwordx2 v[4:5], v148, s[28:31], 0 offen sc1\n"
    "s_mov_b64 exec, -1\n"
    "s_waitcnt lgkmcnt(12)\n"
    "v_mfma_f32_16x16x32_bf16 v[104:107], v[236:239], v[58:61], v[104:107]\n"
    "s_waitcnt vmcnt(7)\n"
    "s_barrier\n"
    "ds_read_b64_tr_b16 v[200:201], v92 offset:12288\n"
    "ds_read_b64_tr_b16 v[202:203], v92 offset:13056\n"
    "s_mov_b64 vcc, s[64:65]\n"
    "s_waitcnt vmcnt(5)\n"
    "v_cndmask_b32_dpp v54, v66, v62, vcc quad_perm:[1,0,3,2] row_mask:0xf bank_mask:0xf\n"
    "v_cndmask_b32_dpp v55, v67, v63, vcc quad_perm:[1,0,3,2] row_mask:0xf bank_mask:0xf\n"
    "v_cndmask_b32_dpp v56, v68, v64, vcc quad_perm:[1,0,3,2] row_mask:0xf bank_mask:0xf\n"
    "v_cndmask_b32_dpp v57, v69, v65, vcc quad_perm:[1,0,3,2] row_mask:0xf bank_mask:0xf\n"
    "s_waitcnt lgkmcnt(12)\n"
    "v_mfma_f32_16x16x32_bf16 v[108:111], v[240:243], v[58:61], v[108:111]\n"
    "ds_read_b64_tr_b16 v[204:205], v92 offset:13824\n"
    "ds_read_b64_tr_b16 v[206:207], v92 offset:14592\n"
    "s_waitcnt lgkmcnt(12)\n"
    "v_mfma_f32_16x16x32_bf16 v[112:115], v[244:247], v[58:61], v[112:115]\n"
    "ds_read_b64_tr_b16 v[208:209], v92 offset:24576\n"
    "ds_read_b64_tr_b16 v[210:211], v92 offset:25344\n"
    "s_mov_b64 vcc, s[66:67]\n"
    "s_nop 0\n"
    "v_cndmask_b32_dpp v66, v62, v66, vcc quad_perm:[1,0,3,2] row_mask:0xf bank_mask:0xf\n"
    "v_cndmask_b32_dpp v67, v63, v67, vcc quad_perm:[1,0,3,2] row_mask:0xf bank_mask:0xf\n"
    "v_cndmask_b32_dpp v68, v64, v68, vcc quad_perm:[1,0,3,2] row_mask:0xf bank_mask:0xf\n"
    "v_cndmask_b32_dpp v69, v65, v69, vcc quad_perm:[1,0,3,2] row_mask:0xf bank_mask:0xf\n"
    "s_waitcnt lgkmcnt(12)\n"
    "v_mfma_f32_16x16x32_bf16 v[116:119], v[248:251], v[58:61], v[116:119]\n"
    "ds_read_b64_tr_b16 v[212:213], v92 offset:26112\n"
    "ds_read_b64_tr_b16 v[214:215], v92 offset:26880\n"
    "v_lshlrev_b32_e32 v11, 4, v3\n"
    "v_add_u32_e32 v11, 77824, v11\n"
    "s_waitcnt lgkmcnt(12)\n"
    "v_mfma_f32_16x16x32_bf16 v[120:123], v[252:255], v[58:61], v[120:123]\n"
    "ds_read_b64_tr_b16 v[216:217], v93 offset:0\n"
    "ds_read_b64_tr_b16 v[218:219], v93 offset:768\n"
    "s_lshl_b32 s52, s56, 6\n"
    "v_mfma_f32_16x16x32_bf16 v[172:175], v[12:15], v[54:57], v[172:175]\n"
    "v_add_u32_e32 v86, s52, v11\n"
    "s_lshl_b32 s52, s57, 6\n"
    "v_mfma_f32_16x16x32_bf16 v[164:167], v[54:57], v[54:57], v[164:167]\n"
    "s_mov_b64 vcc, s[64:65]\n"
    "s_waitcnt vmcnt(3)\n"
    "v_cndmask_b32_dpp v62, v74, v70, vcc quad_perm:[1,0,3,2] row_mask:0xf bank_mask:0xf\n"
    "v_cndmask_b32_dpp v63, v75, v71, vcc quad_perm:[1,0,3,2] row_mask:0xf bank_mask:0xf\n"
    "v_cndmask_b32_dpp v64, v76, v72, vcc quad_perm:[1,0,3,2] row_mask:0xf bank_mask:0xf\n"
    "v_cndmask_b32_dpp v65, v77, v73, vcc quad_perm:[1,0,3,2] row_mask:0xf bank_mask:0xf\n"
    "v_add_u32_e32 v87, s52, v11\n"
    "s_waitcnt lgkmcnt(12)\n"
    "v_mfma_f32_16x16x32_bf16 v[168:171], v[192:195], v[12:15], v[168:171]\n"
    "s_lshl_b32 s52, s58, 6\n"
    "v_add_u32_e32 v88, s52, v11\n"
    "v_mfma_f32_16x16x32_bf16 v[100:103], v[192:195], v[54:57], v[100:103]\n"
    "ds_read_b64_tr_b16 v[220:221], v93 offset:1536\n"
    "ds_read_b64_tr_b16 v[222:223], v93 offset:2304\n"
    "s_mov_b64 vcc, s[66:67]\n"
    "s_nop 0\n"
    "v_cndmask_b32_dpp v74, v70, v74, vcc quad_perm:[1,0,3,2] row_mask:0xf bank_mask:0xf\n"
    "v_cndmask_b32_dpp v75, v71, v75, vcc quad_perm:[1,0,3,2] row_mask:0xf bank_mask:0xf\n"
    "v_cndmask_b32_dpp v76, v72, v76, vcc quad_perm:[1,0,3,2] row_mask:0xf bank_mask:0xf\n"
    "v_cndmask_b32_dpp v77, v73, v77, vcc quad_perm:[1,0,3,2] row_mask:0xf bank_mask:0xf\n"
    "s_lshl_b32 s52, s59, 6\n"
    "v_mfma_f32_16x16x32_bf16 v[172:175], v[12:15], v[66:69], v[172:175]\n"
    "v_add_u32_e32 v89, s52, v11\n"
    "v_mfma_f32_16x16x32_bf16 v[164:167], v[66:69], v[66:69], v[164:167]\n"
    "s_lshl_b32 s52, s60, 6\n"
    "v_add_u32_e32 v90, s52, v11\n"
    "s_waitcnt lgkmcnt(12)\n"
    "v_mfma_f32_16x16x32_bf16 v[168:171], v[196:199], v[12:15], v[168:171]\n"
    "s_add_u32 m0, s44, 0\n"
    "s_nop 0\n"
    "buffer_load_dwordx4 v6, s[16:19], s46 offen offset:0  lds\n"
    "s_lshl_b32 s52, s61, 6\n"
    "v_mfma_f32_16x16x32_bf16 v[100:103], v[196:199], v[66:69], v[100:103]\n"
    "ds_read_b64_tr_b16 v[224:225], v93 offset:12288\n"
    "ds_read_b64_tr_b16 v[226:227], v93 offset:13056\n"
    "v_add_u32_e32 v91, s52, v11\n"
    "v_lshrrev_b32_e32 v11, 1, v3\n"
    "v_mfma_f32_16x16x32_bf16 v[172:175], v[12:15], v[62:65], v[172:175]\n"
    "v_lshlrev_b32_e32 v11, 8, v11\n"
    "v_mfma_f32_16x16x32_bf16 v[164:167], v[62:65], v[62:65], v[164:167]\n"
    "s_mov_b64 vcc, s[64:65]\n"
    "s_waitcnt vmcnt(2)\n"
    "v_cndmask_b32_dpp v70, v82, v78, vcc quad_perm:[1,0,3,2] row_mask:0xf bank_mask:0xf\n"
    "v_cndmask_b32_dpp v71, v83, v79, vcc quad_perm:[1,0,3,2] row_mask:0xf bank_mask:0xf\n"
    "v_cndmask_b32_dpp v72, v84, v80, vcc quad_perm:[1,0,3,2] row_mask:0xf bank_mask:0xf\n"
    "v_cndmask_b32_dpp v73, v85, v81, vcc quad_perm:[1,0,3,2] row_mask:0xf bank_mask:0xf\n"
    "v_lshl_add_u32 v11, v2, 4, v11\n"
    "s_waitcnt lgkmcnt(12)\n"
    "v_mfma_f32_16x16x32_bf16 v[168:171], v[200:203], v[12:15], v[168:171]\n"
    "s_add_u32 m0, s44, 8192\n"
    "s_nop 0\n"
    "buffer_load_dwordx4 v7, s[20:23], s46 offen offset:0  lds\n"
    "s_add_u32 s46, s46, 114688\n"
    "s_mov_b32 s52, 0xffff0000\n"
    "s_mov_b32 s53, 0xffff0000\n"
    "v_mfma_f32_16x16x32_bf16 v[100:103], v[200:203], v[62:65], v[100:103]\n"
    "ds_read_b64_tr_b16 v[228:229], v93 offset:13824\n"
    "ds_read_b64_tr_b16 v[230:231], v93 offset:14592\n"
    "s_mov_b64 vcc, s[66:67]\n"
    "s_nop 0\n"
    "v_cndmask_b32_dpp v82, v78, v82, vcc quad_perm:[1,0,3,2] row_mask:0xf bank_mask:0xf\n"
    "v_cndmask_b32_dpp v83, v79, v83, vcc quad_perm:[1,0,3,2] row_mask:0xf bank_mask:0xf\n"
    "v_cndmask_b32_dpp v84, v80, v84, vcc quad_perm:[1,0,3,2] row_mask:0xf bank_mask:0xf\n"
    "v_cndmask_b32_dpp v85, v81, v85, vcc quad_perm:[1,0,3,2] row_mask:0xf bank_mask:0xf\n"
    "v_mov_b32_e32 v17, s56\n"
    "v_mfma_f32_16x16x32_bf16 v[172:175], v[12:15], v[74:77], v[172:175]\n"
    "v_mov_b32_e32 v18, s57\n"
    "v_cndmask_b32_e64 v17, v17, v18, s[52:53]\n"
    "v_mfma_f32_16x16x32_bf16 v[164:167], v[74:77], v[74:77], v[164:167]\n"
    "v_lshl_add_u32 v26, v17, 9, v11\n"
    "s_waitcnt lgkmcnt(12)\n"
    "v_mfma_f32_16x16x32_bf16 v[168:171], v[204:207], v[12:15], v[168:171]\n"
    "s_add_u32 m0, s44, 16384\n"
    "s_nop 0\n"
    "buffer_load_dwordx4 v6, s[16:19], s46 offen offset:0  lds\n"
    "v_mov_b32_e32 v17, s58\n"
    "v_mov_b32_e32 v18, s59\n"
    "v_mfma_f32_16x16x32_bf16 v[100:103], v[204:207], v[74:77], v[100:103]\n"
    "ds_read_b64_tr_b16 v[232:233], v93 offset:24576\n"
    "ds_read_b64_tr_b16 v[234:235], v93 offset:25344\n"
    "v_cndmask_b32_e64 v17, v17, v18, s[52:53]\n"
    "v_mfma_f32_16x16x32_bf16 v[172:175], v[12:15], v[70:73], v[172:175]\n"
    "v_lshl_add_u32 v27, v17, 9, v11\n"
    "v_mfma_f32_16x16x32_bf16 v[164:167], v[70:73], v[70:73], v[164:167]\n"
    "v_mov_b32_e32 v17, s60\n"
    "v_mov_b32_e32 v18, s61\n"
    "s_waitcnt lgkmcnt(12)\n"
    "v_mfma_f32_16x16x32_bf16 v[168:171], v[208:211], v[12:15], v[168:171]\n"
    "v_cndmask_b32_e64 v17, v17, v18, s[52:53]\n"
    "v_mfma_f32_16x16x32_bf16 v[100:103], v[208:211], v[70:73], v[100:103]\n"
    "ds_read_b64_tr_b16 v[236:237], v93 offset:26112\n"
    "ds_read_b64_tr_b16 v[238:239], v93 offset:26880\n"
    "s_add_u32 m0, s44, 24576\n"
    "s_nop 0\n"
    "buffer_load_dwordx4 v7, s[20:23], s46 offen offset:0  lds\n"
    "s_add_u32 s46, s46, 114688\n"
    "v_lshl_add_u32 v28, v17, 9, v11\n"
    "s_mov_b32 s48, 0x00010001\n"
    "v_mfma_f32_16x16x32_bf16 v[172:175], v[12:15], v[82:85], v[172:175]\n"
    "s_mov_b32 s49, 0x00010001\n"
    "v_mfma_f32_16x16x32_bf16 v[164:167], v[82:85], v[82:85], v[164:167]\n"
    "v_mov_b32_e32 v16, 0\n"
    "s_waitcnt lgkmcnt(12)\n"
    "v_mfma_f32_16x16x32_bf16 v[168:171], v[212:215], v[12:15], v[168:171]\n"
    "v_mov_b32_e32 v11, 0x3b124925\n"
    "v_mfma_f32_16x16x32_bf16 v[100:103], v[212:215], v[82:85], v[100:103]\n"
    "ds_read_b64_tr_b16 v[240:241], v94 offset:0\n"
    "ds_read_b64_tr_b16 v[242:243], v94 offset:768\n"
    "s_add_u32 m0, s44, 32768\n"
    "s_nop 0\n"
    "buffer_load_dwordx4 v6, s[16:19], s46 offen offset:0  lds\n"
    "s_waitcnt lgkmcnt(12)\n"
    "v_mfma_f32_16x16x32_bf16 v[104:107], v[216:219], v[54:57], v[104:107]\n"
    "ds_read_b64_tr_b16 v[244:245], v94 offset:1536\n"
    "ds_read_b64_tr_b16 v[246:247], v94 offset:2304\n"
    "s_mov_b32 s52, 0x100001\n"
    "s_mov_b32 s53, 0x10000100\n"
    "v_cndmask_b32_e64 v16, v16, v164, s[52:53]\n"
    "s_mov_b32 s52, 0x200002\n"
    "s_mov_b32 s53, 0x20000200\n"
    "v_cndmask_b32_e64 v16, v16, v165, s[52:53]\n"
    "s_mov_b32 s52, 0x400004\n"
    "s_mov_b32 s53, 0x40000400\n"
    "v_cndmask_b32_e64 v16, v16, v166, s[52:53]\n"
    "s_waitcnt lgkmcnt(12)\n"
    "v_mfma_f32_16x16x32_bf16 v[104:107], v[220:223], v[66:69], v[104:107]\n"
    "ds_read_b64_tr_b16 v[248:249], v94 offset:12288\n"
    "ds_read_b64_tr_b16 v[250:251], v94 offset:13056\n"
    "s_mov_b64 exec, s[48:49]\n"
    "ds_write_b128 v86, v[168:171] offset:0\n"
    "s_mov_b64 exec, -1\n"
    "s_mov_b32 s52, 0x800008\n"
    "s_mov_b32 s53, 0x80000800\n"
    "v_cndmask_b32_e64 v16, v16, v167, s[52:53]\n"
    "v_mul_f32_e32 v18, 0xbb124925, v172\n"
    "s_waitcnt lgkmcnt(13)\n"
    "v_mfma_f32_16x16x32_bf16 v[104:107], v[224:227], v[62:65], v[104:107]\n"
    "ds_read_b64_tr_b16 v[252:253], v94 offset:13824\n"
    "ds_read_b64_tr_b16 v[254:255], v94 offset:14592\n"
    "v_mov_b32_e32 v17, v16\n"
    "s_nop 1\n"
    "v_permlane32_swap_b32_e32 v16, v17\n"
    "v_add_f32_e32 v16, v16, v17\n"
    "s_waitcnt lgkmcnt(13)\n"
    "v_mfma_f32_16x16x32_bf16 v[104:107], v[228:231], v[74:77], v[104:107]\n"
    "ds_read_b64_tr_b16 v[192:193], v94 offset:24576\n"
    "ds_read_b64_tr_b16 v[194:195], v94 offset:25344\n"
    "s_add_u32 m0, s44, 40960\n"
    "s_nop 0\n"
    "buffer_load_dwordx4 v7, s[20:23], s46 offen offset:0  lds\n"
    "s_add_u32 s46, s46, 114688\n"
    "v_mov_b32_e32 v17, v16\n"
    "s_nop 1\n"
    "v_permlane16_swap_b32_e32 v16, v17\n"
    "s_waitcnt lgkmcnt(13)\n"
    "v_mfma_f32_16x16x32_bf16 v[104:107], v[232:235], v[70:73], v[104:107]\n"
    "ds_read_b64_tr_b16 v[196:197], v94 offset:26112\n"
    "ds_read_b64_tr_b16 v[198:199], v94 offset:26880\n"
    "v_add_f32_e32 v16, v16, v17\n"
    "v_mul_f32_e32 v17, v18, v18\n"
    "v_fma_f32 v16, v16, v11, -v17\n"
    "s_waitcnt lgkmcnt(6)\n"
    "s_barrier\n"
    "ds_read_b128 v[30:33], v86 offset:0\n"
    "ds_read_b128 v[34:37], v87 offset:0\n"
    "ds_read_b128 v[38:41], v88 offset:0\n"
    "ds_read_b128 v[42:45], v89 offset:0\n"
    "ds_read_b128 v[46:49], v90 offset:0\n"
    "ds_read_b128 v[50:53], v91 offset:0\n"
    "v_mfma_f32_16x16x32_bf16 v[104:107], v[236:239], v[82:85], v[104:107]\n"
    "ds_read_b64_tr_b16 v[200:201], v95 offset:0\n"
    "ds_read_b64_tr_b16 v[202:203], v95 offset:768\n"
    "v_max_f32_e32 v16, 0, v16\n"
    "v_add_f32_e32 v16, 0x3727c5ac, v16\n"
    "v_mfma_f32_16x16x32_bf16 v[108:111], v[240:243], v[54:57], v[108:111]\n"
    "ds_read_b64_tr_b16 v[204:205], v95 offset:1536\n"
    "ds_read_b64_tr_b16 v[206:207], v95 offset:2304\n"
    "v_rsq_f32_e32 v16, v16\n"
    "s_nop 0\n"
    "v_mul_f32_e32 v17, v18, v16\n"
    "v_mfma_f32_16x16x32_bf16 v[108:111], v[244:247], v[66:69], v[108:111]\n"
    "ds_read_b64_tr_b16 v[208:209], v95 offset:12288\n"
    "ds_read_b64_tr_b16 v[210:211], v95 offset:13056\n"
    "v_mfma_f32_16x16x32_bf16 v[108:111], v[248:251], v[62:65], v[108:111]\n"
    "ds_read_b64_tr_b16 v[212:213], v95 offset:13824\n"
    "ds_read_b64_tr_b16 v[214:215], v95 offset:14592\n"
    "s_add_u32 m0, s44, 49152\n"
    "s_nop 0\n"
    "buffer_load_dwordx4 v6, s[16:19], s46 offen offset:0  lds\n"
    "s_waitcnt lgkmcnt(13)\n"
    "v_mul_f32_e32 v30, v30, v17\n"
    "v_mul_f32_e32 v31, v31, v17\n"
    "v_mul_f32_e32 v32, v32, v17\n"
    "v_mul_f32_e32 v33, v33, v17\n"
    "v_mfma_f32_16x16x32_bf16 v[108:111], v[252:255], v[74:77], v[108:111]\n"
    "ds_read_b64_tr_b16 v[216:217], v95 offset:24576\n"
    "ds_read_b64_tr_b16 v[218:219], v95 offset:25344\n"
    "s_waitcnt lgkmcnt(14)\n"
    "v_mul_f32_e32 v34, v34, v17\n"
    "v_mul_f32_e32 v35, v35, v17\n"
    "v_mul_f32_e32 v36, v36, v17\n"
    "v_mul_f32_e32 v37, v37, v17\n"
    "v_mfma_f32_16x16x32_bf16 v[108:111], v[192:195], v[70:73], v[108:111]\n"
    "ds_read_b64_tr_b16 v[220:221], v95 offset:26112\n"
    "ds_read_b64_tr_b16 v[222:223], v95 offset:26880\n"
    "v_fma_f32 v30, v100, v16, v30\n"
    "v_fma_f32 v31, v101, v16, v31\n"
    "v_fma_f32 v32, v102, v16, v32\n"
    "v_fma_f32 v33, v103, v16, v33\n"
    "v_mfma_f32_16x16x32_bf16 v[108:111], v[196:199], v[82:85], v[108:111]\n"
    "ds_read_b64_tr_b16 v[224:225], v96 offset:0\n"
    "ds_read_b64_tr_b16 v[226:227], v96 offset:768\n"
    "v_fma_f32 v34, v104, v16, v34\n"
    "v_fma_f32 v35, v105, v16, v35\n"
    "v_fma_f32 v36, v106, v16, v36\n"
    "v_fma_f32 v37, v107, v16, v37\n"
    "s_waitcnt lgkmcnt(12)\n"
    "v_mfma_f32_16x16x32_bf16 v[112:115], v[200:203], v[54:57], v[112:115]\n"
    "ds_read_b64_tr_b16 v[228:229], v96 offset:1536\n"
    "ds_read_b64_tr_b16 v[230:231], v96 offset:2304\n"
    "s_add_u32 m0, s44, 57344\n"
    "s_nop 0\n"
    "buffer_load_dwordx4 v7, s[20:23], s46 offen offset:0  lds\n"
    "s_add_u32 s46, s46, 114688\n"
    "v_cvt_pk_bf16_f32 v78, v30, v31\n"
    "v_cvt_pk_bf16_f32 v80, v34, v35\n"
    "v_cvt_pk_bf16_f32 v79, v32, v33\n"
    "v_cvt_pk_bf16_f32 v81, v36, v37\n"
    "s_waitcnt lgkmcnt(12)\n"
    "v_mfma_f32_16x16x32_bf16 v[112:115], v[204:207], v[66:69], v[112:115]\n"
    "ds_read_b64_tr_b16 v[232:233], v96 offset:12288\n"
    "ds_read_b64_tr_b16 v[234:235], v96 offset:13056\n"
    "v_min_u32_e32 v78, -2, v78\n"
    "v_min_u32_e32 v79, -2, v79\n"
    "v_min_u32_e32 v80, -2, v80\n"
    "v_min_u32_e32 v81, -2, v81\n"
    "s_waitcnt lgkmcnt(12)\n"
    "v_mfma_f32_16x16x32_bf16 v[112:115], v[208:211], v[62:65], v[112:115]\n"
    "ds_read_b64_tr_b16 v[236:237], v96 offset:13824\n"
    "ds_read_b64_tr_b16 v[238:239], v96 offset:14592\n"
    "v_permlane16_swap_b32_e32 v78, v80\n"
    "v_permlane16_swap_b32_e32 v79, v81\n"
    "buffer_store_dwordx4 v[78:81], v26, s[28:31], s54 offen sc1\n"
    "v_mul_f32_e32 v38, v38, v17\n"
    "s_waitcnt lgkmcnt(12)\n"
    "v_mfma_f32_16x16x32_bf16 v[112:115], v[212:215], v[74:77], v[112:115]\n"
    "ds_read_b64_tr_b16 v[240:241], v96 offset:24576\n"
    "ds_read_b64_tr_b16 v[242:243], v96 offset:25344\n"
    "v_mul_f32_e32 v39, v39, v17\n"
    "v_mul_f32_e32 v40, v40, v17\n"
    "v_mul_f32_e32 v41, v41, v17\n"
    "v_mul_f32_e32 v42, v42, v17\n"
    "s_waitcnt lgkmcnt(12)\n"
    "v_mfma_f32_16x16x32_bf16 v[112:115], v[216:219], v[70:73], v[112:115]\n"
    "ds_read_b64_tr_b16 v[244:245], v96 offset:26112\n"
    "ds_read_b64_tr_b16 v[246:247], v96 offset:26880\n"
    "v_mul_f32_e32 v43, v43, v17\n"
    "v_mul_f32_e32 v44, v44, v17\n"
    "v_mul_f32_e32 v45, v45, v17\n"
    "v_mul_f32_e32 v46, v46, v17\n"
    "s_waitcnt lgkmcnt(12)\n"
    "v_mfma_f32_16x16x32_bf16 v[112:115], v[220:223], v[82:85], v[112:115]\n"
    "ds_read_b64_tr_b16 v[248:249], v97 offset:0\n"
    "ds_read_b64_tr_b16 v[250:251], v97 offset:768\n"
    "buffer_load_dwordx2 v[20:21], v19, s[28:31], 0 offen sc1\n"
    "v_mul_f32_e32 v47, v47, v17\n"
    "v_mul_f32_e32 v48, v48, v17\n"
    "v_mul_f32_e32 v49, v49, v17\n"
    "v_mul_f32_e32 v50, v50, v17\n"
    "s_waitcnt lgkmcnt(12)\n"
    "v_mfma_f32_16x16x32_bf16 v[116:119], v[224:227], v[54:57], v[116:119]\n"
    "ds_read_b64_tr_b16 v[252:253], v97 offset:1536\n"
    "ds_read_b64_tr_b16 v[254:255], v97 offset:2304\n"
    "v_mul_f32_e32 v51, v51, v17\n"
    "v_mul_f32_e32 v52, v52, v17\n"
    "v_mul_f32_e32 v53, v53, v17\n"
    "s_waitcnt lgkmcnt(12)\n"
    "v_mfma_f32_16x16x32_bf16 v[116:119], v[228:231], v[66:69], v[116:119]\n"
    "ds_read_b64_tr_b16 v[192:193], v97 offset:12288\n"
    "ds_read_b64_tr_b16 v[194:195], v97 offset:13056\n"
    "v_fma_f32 v38, v108, v16, v38\n"
    "v_fma_f32 v39, v109, v16, v39\n"
    "v_fma_f32 v40, v110, v16, v40\n"
    "s_waitcnt lgkmcnt(12)\n"
    "v_mfma_f32_16x16x32_bf16 v[116:119], v[232:235], v[62:65], v[116:119]\n"
    "ds_read_b64_tr_b16 v[196:197], v97 offset:13824\n"
    "ds_read_b64_tr_b16 v[198:199], v97 offset:14592\n"
    "v_fma_f32 v41, v111, v16, v41\n"
    "v_fma_f32 v42, v112, v16, v42\n"
    "s_waitcnt lgkmcnt(12)\n"
    "v_mfma_f32_16x16x32_bf16 v[116:119], v[236:239], v[74:77], v[116:119]\n"
    "ds_read_b64_tr_b16 v[200:201], v97 offset:24576\n"
    "ds_read_b64_tr_b16 v[202:203], v97 offset:25344\n"
    "v_fma_f32 v43, v113, v16, v43\n"
    "v_fma_f32 v44, v114, v16, v44\n"
    "v_fma_f32 v45, v115, v16, v45\n"
    "s_waitcnt lgkmcnt(12)\n"
    "v_mfma_f32_16x16x32_bf16 v[116:119], v[240:243], v[70:73], v[116:119]\n"
    "ds_read_b64_tr_b16 v[204:205], v97 offset:26112\n"
    "ds_read_b64_tr_b16 v[206:207], v97 offset:26880\n"
    "v_cvt_pk_bf16_f32 v58, v38, v39\n"
    "v_cvt_pk_bf16_f32 v60, v42, v43\n"
    "s_waitcnt lgkmcnt(12)\n"
    "v_mfma_f32_16x16x32_bf16 v[116:119], v[244:247], v[82:85], v[116:119]\n"
    "v_cvt_pk_bf16_f32 v59, v40, v41\n"
    "v_cvt_pk_bf16_f32 v61, v44, v45\n"
    "s_waitcnt lgkmcnt(10)\n"
    "v_mfma_f32_16x16x32_bf16 v[120:123], v[248:251], v[54:57], v[120:123]\n"
    "v_min_u32_e32 v58, -2, v58\n"
    "v_min_u32_e32 v59, -2, v59\n"
    "v_min_u32_e32 v60, -2, v60\n"
    "s_waitcnt lgkmcnt(8)\n"
    "v_mfma_f32_16x16x32_bf16 v[120:123], v[252:255], v[66:69], v[120:123]\n"
    "v_min_u32_e32 v61, -2, v61\n"
    "v_permlane16_swap_b32_e32 v58, v60\n"
    "s_waitcnt lgkmcnt(6)\n"
    "v_mfma_f32_16x16x32_bf16 v[120:123], v[192:195], v[62:65], v[120:123]\n"
    "v_permlane16_swap_b32_e32 v59, v61\n"
    "buffer_store_dwordx4 v[58:61], v27, s[28:31], s54 offen sc1\n"
    "s_waitcnt lgkmcnt(4)\n"
    "v_mfma_f32_16x16x32_bf16 v[120:123], v[196:199], v[74:77], v[120:123]\n"
    "s_waitcnt lgkmcnt(2)\n"
    "v_mfma_f32_16x16x32_bf16 v[120:123], v[200:203], v[70:73], v[120:123]\n"
    "s_waitcnt lgkmcnt(0)\n"
    "v_mfma_f32_16x16x32_bf16 v[120:123], v[204:207], v[82:85], v[120:123]\n"
    "v_fma_f32 v46, v116, v16, v46\n"
    "v_fma_f32 v47, v117, v16, v47\n"
    "v_fma_f32 v48, v118, v16, v48\n"
    "v_fma_f32 v49, v119, v16, v49\n"
    "s_nop 3\n"
    "v_fma_f32 v50, v120, v16, v50\n"
    "v_fma_f32 v51, v121, v16, v51\n"
    "v_fma_f32 v52, v122, v16, v52\n"
    "v_fma_f32 v53, v123, v16, v53\n"
    "v_cvt_pk_bf16_f32 v160, v46, v47\n"
    "v_cvt_pk_bf16_f32 v162, v50, v51\n"
    "v_cvt_pk_bf16_f32 v161, v48, v49\n"
    "v_cvt_pk_bf16_f32 v163, v52, v53\n"
    "v_min_u32_e32 v160, -2, v160\n"
    "v_min_u32_e32 v161, -2, v161\n"
    "v_min_u32_e32 v162, -2, v162\n"
    "v_min_u32_e32 v163, -2, v163\n"
    "s_nop 0\n"
    "v_permlane16_swap_b32_e32 v160, v162\n"
    "v_permlane16_swap_b32_e32 v161, v163\n"
    "buffer_store_dwordx4 v[160:163], v28, s[28:31], s54 offen sc1\n"
    "s_waitcnt vmcnt(4)\n"
    "s_waitcnt vmcnt(2)\n"
    "v_cmp_ne_u64_e32 vcc, s[36:37], v[20:21]\n"
    "s_nop 4\n"
    "s_cbranch_vccz .Lrt1x\n"
    ".Lrt1:\n"
    "s_cmp_eq_u32 s100, 0\n"
    "s_cbranch_scc1 .Lrt1x\n"
    "s_sub_u32 s100, s100, 1\n"
    "s_mov_b64 exec, vcc\n"
    "buffer_load_dwordx2 v[20:21], v19, s[28:31], 0 offen sc1\n"
    "s_mov_b64 exec, -1\n"
    "s_waitcnt vmcnt(0)\n"
    "v_cmp_ne_u64_e32 vcc, s[36:37], v[20:21]\n"
    "s_nop 4\n"
    "s_cbranch_vccnz .Lrt1\n"
    ".Lrt1x:\n"
    "s_branch .Lj1\n"
    ".Lhelper0:\n"
    "s_waitcnt lgkmcnt(0)\n"
    "s_load_dwordx2 s[36:37], s[28:29], 0x0\n"
    "v_mov_b32_e32 v144, -1\n"
    "v_mov_b32_e32 v145, -1\n"
    "v_mov_b32_e32 v146, -1\n"
    "v_mov_b32_e32 v147, -1\n"
    "s_sub_u32 s48, s2, 224\n"
    "s_lshl_b32 s49, s48, 3\n"
    "s_add_u32 s50, s49, s3\n"
    "s_mov_b32 s70, s50\n"
    "v_min_u32_e32 v11, 13, v0\n"
    "v_lshlrev_b32_e32 v11, 7, v11\n"
    "s_mul_i32 s52, s70, 1792\n"
    "s_add_u32 s52, s52, 0\n"
    "buffer_load_dword v152, v11, s[16:19], s52 offen\n"
    "buffer_load_dword v153, v11, s[20:23], s52 offen\n"
    "v_min_u32_e32 v11, 24, v0\n"
    "v_lshlrev_b32_e32 v11, 7, v11\n"
    "s_mul_i32 s52, s70, 3200\n"
    "s_add_u32 s52, s52, 458752\n"
    "buffer_load_dword v154, v11, s[16:19], s52 offen\n"
    "buffer_load_dword v155, v11, s[20:23], s52 offen\n"
    "s_lshl_b32 s50, s42, 3\n"
    "s_add_u32 s50, s50, s3\n"
    "s_lshl_b32 s101, s38, 7\n"
    "s_add_u32 s101, s101, s50\n"
    "s_lshl_b32 s101, s101, 3\n"
    "s_add_u32 s101, s101, 4992\n"
    "s_lshl_b32 s50, s50, 10\n"
    "s_lshl_b32 s48, s38, 17\n"
    "s_add_u32 s48, s48, 9437184\n"
    "s_add_u32 s49, s48, 131072\n"
    "s_and_b32 s51, s38, 7\n"
    "s_lshl_b32 s51, s51, 14\n"
    "s_add_u32 s51, s51, s50\n"
    "s_add_u32 s48, s48, s51\n"
    "s_mul_i32 s9, s38, 458752\n"
    "s_add_u32 s9, s9, 11534336\n"
    "s_add_u32 s10, s9, 458752\n"
    "s_add_u32 s9, s9, s51\n"
    "s_sleep 16\n"
    "s_sleep 16\n"
    "s_sleep 16\n"
    "s_sleep 16\n"
    "s_sleep 16\n"
    "s_sleep 16\n"
    "s_sleep 16\n"
    "s_lshl_b32 s71, s40, 3\n"
    "s_add_u32 s71, s71, s3\n"
    "s_lshl_b32 s71, s71, 4\n"
    "s_add_u32 s71, s71, s41\n"
    "s_lshl_b32 s47, s71, 10\n"
    "s_add_u32 s47, s47, 7340032\n"
    "buffer_store_dwordx4 v[144:147], v1, s[28:31], s47 offen sc1\n"
    "s_lshl_b32 s71, s71, 3\n"
    "s_add_u32 s71, s71, 36864\n"
    "v_mov_b32_e32 v148, s101\n"
    "v_writelane_b32 v148, s71, 1\n"
    "buffer_store_dwordx4 v[144:147], v1, s[28:31], s48 offen sc1\n"
    "s_add_u32 s48, s48, 16384\n"
    "s_cmp_ge_u32 s48, s49\n"
    "s_cselect_b32 s55, 131072, 0\n"
    "s_sub_u32 s48, s48, s55\n"
    "buffer_store_dwordx4 v[144:147], v1, s[28:31], s48 offen sc1\n"
    "s_add_u32 s48, s48, 16384\n"
    "s_cmp_ge_u32 s48, s49\n"
    "s_cselect_b32 s55, 131072, 0\n"
    "s_sub_u32 s48, s48, s55\n"
    "buffer_store_dwordx4 v[144:147], v1, s[28:31], s48 offen sc1\n"
    "s_add_u32 s48, s48, 16384\n"
    "s_cmp_ge_u32 s48, s49\n"
    "s_cselect_b32 s55, 131072, 0\n"
    "s_sub_u32 s48, s48, s55\n"
    "buffer_store_dwordx4 v[144:147], v1, s[28:31], s48 offen sc1\n"
    "s_add_u32 s48, s48, 16384\n"
    "s_cmp_ge_u32 s48, s49\n"
    "s_cselect_b32 s55, 131072, 0\n"
    "s_sub_u32 s48, s48, s55\n"
    "buffer_store_dwordx4 v[144:147], v1, s[28:31], s48 offen sc1\n"
    "s_add_u32 s48, s48, 16384\n"
    "s_cmp_ge_u32 s48, s49\n"
    "s_cselect_b32 s55, 131072, 0\n"
    "s_sub_u32 s48, s48, s55\n"
    "buffer_store_dwordx4 v[144:147], v1, s[28:31], s48 offen sc1\n"
    "s_add_u32 s48, s48, 16384\n"
    "s_cmp_ge_u32 s48, s49\n"
    "s_cselect_b32 s55, 131072, 0\n"
    "s_sub_u32 s48, s48, s55\n"
    "buffer_store_dwordx4 v[144:147], v1, s[28:31], s48 offen sc1\n"
    "s_add_u32 s48, s48, 16384\n"
    "s_cmp_ge_u32 s48, s49\n"
    "s_cselect_b32 s55, 131072, 0\n"
    "s_sub_u32 s48, s48, s55\n"
    "buffer_store_dwordx4 v[144:147], v1, s[28:31], s48 offen sc1\n"
    "s_add_u32 s48, s48, 16384\n"
    "s_cmp_ge_u32 s48, s49\n"
    "s_cselect_b32 s55, 131072, 0\n"
    "s_sub_u32 s48, s48, s55\n"
    "s_mul_i32 s68, s41, 7\n"
    "s_lshr_b32 s68, s68, 1\n"
    "s_lshl_b32 s71, s40, 3\n"
    "s_add_u32 s69, s71, s3\n"
    "s_mul_i32 s47, s3, 3\n"
    "s_add_u32 s71, s71, s47\n"
    "s_sub_u32 s71, s71, 16\n"
    "v_min_u32_e32 v11, 13, v0\n"
    "v_lshrrev_b32_e32 v16, 1, v11\n"
    "v_add_u32_e32 v16, 2, v16\n"
    "v_mul_u32_u24_e32 v17, 11, v16\n"
    "v_lshrrev_b32_e32 v17, 5, v17\n"
    "v_mul_u32_u24_e32 v18, 3, v17\n"
    "v_sub_u32_e32 v16, v16, v18\n"
    "v_add_u32_e32 v16, s71, v16\n"
    "v_max_i32_e32 v16, 0, v16\n"
    "v_writelane_b32 v16, s69, 0\n"
    "v_writelane_b32 v16, s69, 1\n"
    "v_and_b32_e32 v11, 1, v11\n"
    "v_mul_u32_u24_e32 v11, 3, v11\n"
    "v_mad_u32_u24 v11, v17, 56, v11\n"
    "v_add_u32_e32 v11, s68, v11\n"
    "v_mul_u32_u24_e32 v11, 171, v11\n"
    "v_lshrrev_b32_e32 v11, 11, v11\n"
    "v_lshrrev_b32_e32 v17, 3, v16\n"
    "v_lshl_add_u32 v11, v17, 4, v11\n"
    "v_lshlrev_b32_e32 v11, 6, v11\n"
    "v_and_b32_e32 v16, 7, v16\n"
    "v_lshl_add_u32 v19, v16, 3, v11\n"
    "v_add_u32_e32 v19, 4096, v19\n"
    "v_mov_b32_e32 v12, 0x3f803f80\n"
    "v_mov_b32_e32 v13, 0x3f803f80\n"
    "v_mov_b32_e32 v14, 0x3f803f80\n"
    "v_mov_b32_e32 v15, 0x3f803f80\n"
    "v_mov_b32_e32 v24, 0\n"
    "v_mov_b32_e32 v25, 0\n"
    "s_lshl_b32 s52, s3, 2\n"
    "v_add_u32_e32 v11, s52, v3\n"
    "v_mul_u32_u24_e32 v6, 3584, v11\n"
    "v_lshrrev_b32_e32 v11, 1, v2\n"
    "v_xor_b32_e32 v11, v11, v3\n"
    "v_lshl_add_u32 v6, v11, 5, v6\n"
    "v_and_b32_e32 v11, 16, v1\n"
    "v_add_u32_e32 v6, v6, v11\n"
    "v_xor_b32_e32 v7, 0x80, v6\n"
    "v_lshrrev_b32_e32 v11, 2, v2\n"
    "v_and_b32_e32 v17, 1, v3\n"
    "v_lshl_add_u32 v18, v17, 2, v11\n"
    "v_xor_b32_e32 v18, s3, v18\n"
    "v_lshlrev_b32_e32 v8, 5, v18\n"
    "v_lshl_add_u32 v8, v17, 13, v8\n"
    "v_lshl_add_u32 v8, v11, 8, v8\n"
    "v_lshrrev_b32_e32 v18, 5, v0\n"
    "v_lshl_add_u32 v8, v18, 10, v8\n"
    "v_and_b32_e32 v18, 3, v0\n"
    "v_lshl_add_u32 v8, v18, 3, v8\n"
    "v_add_u32_e32 v9, 0x10000, v8\n"
    "v_add_u32_e32 v10, 0x20000, v8\n"
    "s_waitcnt lgkmcnt(0)\n"
    "s_add_u32 s36, s36, 0x7f4a7c15\n"
    "s_addc_u32 s37, s37, 0x9e3779b9\n"
    "v_mov_b32_e32 v4, s36\n"
    "v_mov_b32_e32 v5, s37\n"
    "v_min_u32_e32 v11, 10, v0\n"
    "v_lshlrev_b32_e32 v11, 7, v11\n"
    "s_mul_i32 s52, s70, 1408\n"
    "s_add_u32 s52, s52, 1261568\n"
    "buffer_load_dword v156, v11, s[16:19], s52 offen\n"
    "buffer_load_dword v157, v11, s[20:23], s52 offen\n"
    "v_min_u32_e32 v11, 12, v0\n"
    "v_lshlrev_b32_e32 v11, 7, v11\n"
    "s_mul_i32 s52, s70, 1664\n"
    "s_add_u32 s52, s52, 0\n"
    "buffer_load_dword v158, v11, s[12:15], s52 offen\n"
    "buffer_store_dwordx4 v[144:147], v1, s[28:31], s9 offen sc1\n"
    "s_add_u32 s9, s9, 16384\n"
    "s_cmp_ge_u32 s9, s10\n"
    "s_cselect_b32 s55, 458752, 0\n"
    "s_sub_u32 s9, s9, s55\n"
    "buffer_store_dwordx4 v[144:147], v1, s[28:31], s9 offen sc1\n"
    "s_add_u32 s9, s9, 16384\n"
    "s_cmp_ge_u32 s9, s10\n"
    "s_cselect_b32 s55, 458752, 0\n"
    "s_sub_u32 s9, s9, s55\n"
    "buffer_store_dwordx4 v[144:147], v1, s[28:31], s9 offen sc1\n"
    "s_add_u32 s9, s9, 16384\n"
    "s_cmp_ge_u32 s9, s10\n"
    "s_cselect_b32 s55, 458752, 0\n"
    "s_sub_u32 s9, s9, s55\n"
    "buffer_store_dwordx4 v[144:147], v1, s[28:31], s9 offen sc1\n"
    "s_add_u32 s9, s9, 16384\n"
    "s_cmp_ge_u32 s9, s10\n"
    "s_cselect_b32 s55, 458752, 0\n"
    "s_sub_u32 s9, s9, s55\n"
    "s_mov_b32 s6, 0\n"
    "s_mov_b32 s14, 0\n"
    "s_mov_b32 s18, 0\n"
    "s_mov_b32 s22, 0\n"
    "s_waitcnt vmcnt(7)\n"
    "s_mov_b64 exec, 3\n"
    "buffer_store_dwordx2 v[4:5], v148, s[28:31], 0 offen sc1\n"
    "s_mov_b64 exec, -1\n"
    "buffer_load_dwordx2 v[20:21], v19, s[28:31], 0 offen sc1\n"
    "s_waitcnt vmcnt(0)\n"
    "v_cmp_ne_u64_e32 vcc, s[36:37], v[20:21]\n"
    "s_nop 4\n"
    "s_cbranch_vccz .Lrt2x\n"
    ".Lrt2:\n"
    "s_cmp_eq_u32 s100, 0\n"
    "s_cbranch_scc1 .Lrt2x\n"
    "s_sub_u32 s100, s100, 1\n"
    "s_mov_b64 exec, vcc\n"
    "buffer_load_dwordx2 v[20:21], v19, s[28:31], 0 offen sc1\n"
    "s_mov_b64 exec, -1\n"
    "s_waitcnt vmcnt(0)\n"
    "v_cmp_ne_u64_e32 vcc, s[36:37], v[20:21]\n"
    "s_nop 4\n"
    "s_cbranch_vccnz .Lrt2\n"
    ".Lrt2x:\n"
    ".Lj1:\n"
    "s_barrier\n"
    "ds_read_b64_tr_b16 v[144:145], v8 offset:0\n"
    "s_add_u32 m0, s44, 65536\n"
    "s_nop 0\n"
    "buffer_load_dwordx4 v6, s[16:19], s46 offen offset:0  lds\n"
    "s_mul_i32 s62, s41, 7\n"
    "s_lshr_b32 s62, s62, 1\n"
    "ds_read_b64_tr_b16 v[146:147], v8 offset:4096\n"
    "s_lshl_b32 s52, s40, 3\n"
    "s_add_u32 s63, s52, s3\n"
    "ds_read_b64_tr_b16 v[148:149], v8 offset:2048\n"
    "s_mul_i32 s53, s3, 3\n"
    "s_add_u32 s52, s52, s53\n"
    "ds_read_b64_tr_b16 v[150:151], v8 offset:6144\n"
    "s_sub_u32 s52, s52, 16\n"
    "v_and_b32_e32 v11, 1, v3\n"
    "ds_read_b64_tr_b16 v[152:153], v8 offset:16384\n"
    "v_lshlrev_b32_e32 v11, 7, v11\n"
    "v_xor_b32_e32 v26, v1, v11\n"
    "ds_read_b64_tr_b16 v[154:155], v8 offset:20480\n"
    "v_add_u32_e32 v26, 14336, v26\n"
    "s_lshl_b32 s51, s62, 8\n"
    "ds_read_b64_tr_b16 v[156:157], v8 offset:18432\n"
    "s_add_u32 s51, s51, 1048576\n"
    "s_add_i32 s48, s52, 0\n"
    "ds_read_b64_tr_b16 v[158:159], v8 offset:22528\n"
    "s_max_i32 s48, s48, 0\n"
    "s_mul_i32 s48, s48, 43008\n"
    "ds_read_b64_tr_b16 v[160:161], v8 offset:32768\n"
    "s_add_u32 m0, s44, 73728\n"
    "s_nop 0\n"
    "buffer_load_dwordx4 v7, s[20:23], s46 offen offset:0  lds\n"
    "s_add_u32 s46, s46, 114688\n"
    "s_add_u32 s48, s48, s51\n"
    "s_add_u32 s48, s48, 14336\n"
    "ds_read_b64_tr_b16 v[162:163], v8 offset:36864\n"
    "s_add_i32 s49, s52, 1\n"
    "s_max_i32 s49, s49, 0\n"
    "ds_read_b64_tr_b16 v[164:165], v8 offset:34816\n"
    "s_mul_i32 s49, s49, 43008\n"
    "s_add_u32 s49, s49, s51\n"
    "ds_read_b64_tr_b16 v[166:167], v8 offset:38912\n"
    "s_add_u32 s49, s49, 13312\n"
    "s_add_i32 s50, s52, 2\n"
    "ds_read_b64_tr_b16 v[168:169], v8 offset:49152\n"
    "s_max_i32 s50, s50, 0\n"
    "s_mul_i32 s50, s50, 43008\n"
    "ds_read_b64_tr_b16 v[170:171], v8 offset:53248\n"
    "s_add_u32 s50, s50, s51\n"
    "s_add_u32 s50, s50, 12288\n"
    "ds_read_b64_tr_b16 v[172:173], v8 offset:51200\n"
    "s_mul_i32 s54, s63, 43008\n"
    "s_add_u32 s51, s54, s51\n"
    "ds_read_b64_tr_b16 v[174:175], v8 offset:55296\n"
    "s_lshl_b32 s68, s53, 10\n"
    "v_add_u32_e32 v19, s68, v1\n"
    "buffer_load_dwordx4 v[36:39], v1, s[28:31], s48 offen offset:0 sc1\n"
    "buffer_load_dwordx4 v[40:43], v1, s[28:31], s49 offen offset:1024 sc1\n"
    "buffer_load_dwordx4 v[44:47], v1, s[28:31], s50 offen offset:2048 sc1\n"
    "buffer_load_dwordx4 v[108:111], v1, s[28:31], s51 offen offset:0 sc1\n"
    "buffer_load_dwordx4 v[48:51], v26, s[28:31], s48 offen offset:0 sc1\n"
    "buffer_load_dwordx4 v[52:55], v26, s[28:31], s49 offen offset:1024 sc1\n"
    "buffer_load_dwordx4 v[56:59], v26, s[28:31], s50 offen offset:2048 sc1\n"
    "s_mov_b32 s54, 0xf149f2ca\n"
    "s_waitcnt lgkmcnt(14)\n"
    "v_dot2c_f32_bf16_e32 v24, v12, v144\n"
    "v_mov_b32_e32 v16, s54\n"
    "v_lshl_add_u32 v11, v3, 2, 0\n"
    "v_dot2c_f32_bf16_e32 v25, v12, v145\n"
    "v_cmp_le_u32_e32 vcc, v11, v2\n"
    "s_nop 1\n"
    "v_cndmask_b32_e32 v28, 0, v16, vcc\n"
    "v_dot2c_f32_bf16_e32 v24, v12, v146\n"
    "v_cndmask_b32_e64 v32, v16, 0, vcc\n"
    "v_lshl_add_u32 v11, v3, 2, 1\n"
    "v_dot2c_f32_bf16_e32 v25, v12, v147\n"
    "v_cmp_le_u32_e32 vcc, v11, v2\n"
    "s_nop 1\n"
    "v_cndmask_b32_e32 v29, 0, v16, vcc\n"
    "s_waitcnt lgkmcnt(13)\n"
    "v_dot2c_f32_bf16_e32 v24, v12, v148\n"
    "v_cndmask_b32_e64 v33, v16, 0, vcc\n"
    "v_lshl_add_u32 v11, v3, 2, 2\n"
    "v_dot2c_f32_bf16_e32 v25, v12, v149\n"
    "v_cmp_le_u32_e32 vcc, v11, v2\n"
    "s_nop 1\n"
    "v_cndmask_b32_e32 v30, 0, v16, vcc\n"
    "s_waitcnt lgkmcnt(12)\n"
    "v_dot2c_f32_bf16_e32 v24, v12, v150\n"
    "v_cndmask_b32_e64 v34, v16, 0, vcc\n"
    "v_lshl_add_u32 v11, v3, 2, 3\n"
    "v_dot2c_f32_bf16_e32 v25, v12, v151\n"
    "v_cmp_le_u32_e32 vcc, v11, v2\n"
    "s_nop 1\n"
    "v_cndmask_b32_e32 v31, 0, v16, vcc\n"
    "s_waitcnt lgkmcnt(11)\n"
    "v_dot2c_f32_bf16_e32 v24, v12, v152\n"
    "v_cndmask_b32_e64 v35, v16, 0, vcc\n"
    "v_add_u32_e32 v78, s44, v1\n"
    "v_dot2c_f32_bf16_e32 v25, v12, v153\n"
    "v_lshrrev_b32_e32 v11, 2, v2\n"
    "v_lshl_add_u32 v11, v3, 2, v11\n"
    "s_waitcnt lgkmcnt(10)\n"
    "v_dot2c_f32_bf16_e32 v24, v12, v154\n"
    "v_lshlrev_b32_e32 v11, 4, v11\n"
    "v_bfe_u32 v17, v0, 1, 1\n"
    "v_dot2c_f32_bf16_e32 v25, v12, v155\n"
    "v_mul_u32_u24_e32 v17, 384, v17\n"
    "v_xor_b32_e32 v11, v11, v17\n"
    "s_waitcnt lgkmcnt(9)\n"
    "v_dot2c_f32_bf16_e32 v24, v12, v156\n"
    "v_and_b32_e32 v17, 1, v0\n"
    "v_lshl_add_u32 v11, v17, 3, v11\n"
    "v_dot2c_f32_bf16_e32 v25, v12, v157\n"
    "v_add_u32_e32 v77, s44, v11\n"
    "v_lshrrev_b32_e32 v11, 3, v0\n"
    "s_waitcnt lgkmcnt(8)\n"
    "v_dot2c_f32_bf16_e32 v24, v12, v158\n"
    "v_mul_u32_u24_e32 v11, 896, v11\n"
    "v_bfe_u32 v18, v0, 1, 2\n"
    "v_dot2c_f32_bf16_e32 v25, v12, v159\n"
    "v_xor_b32_e32 v18, v18, v3\n"
    "v_lshl_add_u32 v79, v18, 5, v11\n"
    "s_waitcnt lgkmcnt(7)\n"
    "v_dot2c_f32_bf16_e32 v24, v12, v160\n"
    "v_lshl_add_u32 v79, v17, 4, v79\n"
    "s_lshl_b32 s52, s3, 2\n"
    "v_dot2c_f32_bf16_e32 v25, v12, v161\n"
    "v_add_u32_e32 v18, s52, v3\n"
    "v_xor_b32_e32 v18, v18, v0\n"
    "s_waitcnt lgkmcnt(6)\n"
    "v_dot2c_f32_bf16_e32 v24, v12, v162\n"
    "v_and_b32_e32 v18, 7, v18\n"
    "v_lshl_add_u32 v76, v18, 4, v11\n"
    "v_dot2c_f32_bf16_e32 v25, v12, v163\n"
    "s_bitcmp1_b32 s41, 0\n"
    "s_cselect_b32 s64, 0xffff, 0\n"
    "s_waitcnt lgkmcnt(5)\n"
    "v_dot2c_f32_bf16_e32 v24, v12, v164\n"
    "s_mov_b32 s65, 0\n"
    "s_mov_b32 s66, 0\n"
    "v_dot2c_f32_bf16_e32 v25, v12, v165\n"
    "s_cselect_b32 s67, 0, 0xffff0000\n"
    "s_lshl_b32 s53, s43, 7\n"
    "s_waitcnt lgkmcnt(4)\n"
    "v_dot2c_f32_bf16_e32 v24, v12, v166\n"
    "s_mul_i32 s56, s3, 50176\n"
    "s_add_u32 s56, s56, s53\n"
    "v_dot2c_f32_bf16_e32 v25, v12, v167\n"
    "s_mul_i32 s47, s3, 200704\n"
    "s_add_u32 s47, s47, s53\n"
    "s_waitcnt lgkmcnt(3)\n"
    "v_dot2c_f32_bf16_e32 v24, v12, v168\n"
    "s_lshl_b32 s57, s38, 4\n"
    "s_lshr_b32 s52, s3, 1\n"
    "v_dot2c_f32_bf16_e32 v25, v12, v169\n"
    "s_lshl_b32 s52, s52, 2\n"
    "s_add_u32 s57, s57, s52\n"
    "s_waitcnt lgkmcnt(2)\n"
    "v_dot2c_f32_bf16_e32 v24, v12, v170\n"
    "s_lshl_b32 s52, s42, 1\n"
    "s_add_u32 s57, s57, s52\n"
    "v_dot2c_f32_bf16_e32 v25, v12, v171\n"
    "s_and_b32 s52, s3, 1\n"
    "s_add_u32 s57, s57, s52\n"
    "s_waitcnt lgkmcnt(1)\n"
    "v_dot2c_f32_bf16_e32 v24, v12, v172\n"
    "s_mul_i32 s57, s57, 7168\n"
    "s_add_u32 s57, s57, s53\n"
    "v_dot2c_f32_bf16_e32 v25, v12, v173\n"
    "s_mul_i32 s58, s3, 7168\n"
    "s_add_u32 s58, s58, 49152\n"
    "s_waitcnt lgkmcnt(0)\n"
    "v_dot2c_f32_bf16_e32 v24, v12, v174\n"
    "s_lshl_b32 s60, s63, 4\n"
    "s_add_u32 s60, s60, s41\n"
    "v_dot2c_f32_bf16_e32 v25, v12, v175\n"
    "s_lshl_b32 s59, s60, 10\n"
    "s_add_u32 s59, s59, 7340032\n"
    "v_min_u32_e32 v18, 6, v0\n"
    "v_lshlrev_b32_e32 v18, 7, v18\n"
    "s_lshl_b32 s61, s2, 3\n"
    "s_add_u32 s61, s61, s3\n"
    "s_mul_i32 s61, s61, 896\n"
    "s_waitcnt lgkmcnt(0)\n"
    "s_barrier\n"
    "s_waitcnt vmcnt(6)\n"
    "ds_write_b128 v19, v[36:39] offset:0\n"
    "v_max3_u32 v22, v36, v37, v38\n"
    "s_add_u32 m0, s44, 81920\n"
    "s_nop 0\n"
    "buffer_load_dwordx4 v6, s[16:19], s46 offen offset:0  lds\n"
    "s_waitcnt vmcnt(6)\n"
    "ds_write_b128 v19, v[40:43] offset:1024\n"
    "v_max3_u32 v22, v22, v39, v40\n"
    "v_max3_u32 v22, v22, v41, v42\n"
    "s_add_u32 m0, s44, 90112\n"
    "s_nop 0\n"
    "buffer_load_dwordx4 v7, s[20:23], s46 offen offset:0  lds\n"
    "s_add_u32 s46, s46, 114688\n"
    "s_waitcnt vmcnt(6)\n"
    "ds_write_b128 v19, v[44:47] offset:2048\n"
    "v_max3_u32 v22, v22, v43, v44\n"
    "v_max3_u32 v22, v22, v45, v46\n"
    "s_add_u32 m0, s44, 98304\n"
    "s_nop 0\n"
    "buffer_load_dwordx4 v6, s[16:19], s46 offen offset:0  lds\n"
    "s_waitcnt vmcnt(6)\n"
    "v_max3_u32 v22, v22, v47, v108\n"
    "v_max3_u32 v22, v22, v109, v110\n"
    "s_add_u32 m0, s44, 106496\n"
    "s_nop 0\n"
    "buffer_load_dwordx4 v7, s[20:23], s46 offen offset:0  lds\n"
    "s_add_u32 s46, s46, 114688\n"
    "s_waitcnt vmcnt(6)\n"
    "ds_write_b128 v19, v[48:51] offset:24576\n"
    "v_max3_u32 v22, v22, v111, v48\n"
    "v_max3_u32 v22, v22, v49, v50\n"
    "s_add_u32 m0, s44, 49152\n"
    "s_nop 0\n"
    "buffer_load_dwordx4 v6, s[16:19], s46 offen offset:0  lds\n"
    "s_waitcnt vmcnt(6)\n"
    "ds_write_b128 v19, v[52:55] offset:25600\n"
    "v_max3_u32 v22, v22, v51, v52\n"
    "v_max3_u32 v22, v22, v53, v54\n"
    "s_add_u32 m0, s44, 57344\n"
    "s_nop 0\n"
    "buffer_load_dwordx4 v7, s[20:23], s46 offen offset:0  lds\n"
    "s_add_u32 s46, s46, 114688\n"
    "s_waitcnt vmcnt(6)\n"
    "ds_write_b128 v19, v[56:59] offset:26624\n"
    "v_max3_u32 v22, v22, v55, v56\n"
    "v_max3_u32 v22, v22, v57, v58\n"
    "v_max_u32_e32 v22, v22, v59\n"
    "v_cmp_eq_u32_e32 vcc, -1, v22\n"
    "s_add_u32 m0, s44, 114688\n"
    "s_nop 0\n"
    "buffer_load_dwordx4 v6, s[16:19], s46 offen offset:0  lds\n"
    "s_nop 4\n"
    "s_cbranch_vccz .Lrt3x\n"
    ".Lrt3:\n"
    "s_cmp_eq_u32 s100, 0\n"
    "s_cbranch_scc1 .Lrt3x\n"
    "s_sub_u32 s100, s100, 1\n"
    "s_mov_b64 exec, vcc\n"
    "buffer_load_dwordx4 v[36:39], v1, s[28:31], s48 offen offset:0 sc1\n"
    "buffer_load_dwordx4 v[40:43], v1, s[28:31], s49 offen offset:1024 sc1\n"
    "buffer_load_dwordx4 v[44:47], v1, s[28:31], s50 offen offset:2048 sc1\n"
    "buffer_load_dwordx4 v[108:111], v1, s[28:31], s51 offen offset:0 sc1\n"
    "buffer_load_dwordx4 v[48:51], v26, s[28:31], s48 offen offset:0 sc1\n"
    "buffer_load_dwordx4 v[52:55], v26, s[28:31], s49 offen offset:1024 sc1\n"
    "buffer_load_dwordx4 v[56:59], v26, s[28:31], s50 offen offset:2048 sc1\n"
    "s_mov_b64 exec, -1\n"
    "s_waitcnt vmcnt(0)\n"
    "ds_write_b128 v19, v[36:39] offset:0\n"
    "v_max3_u32 v22, v36, v37, v38\n"
    "ds_write_b128 v19, v[40:43] offset:1024\n"
    "v_max3_u32 v22, v22, v39, v40\n"
    "v_max3_u32 v22, v22, v41, v42\n"
    "ds_write_b128 v19, v[44:47] offset:2048\n"
    "v_max3_u32 v22, v22, v43, v44\n"
    "v_max3_u32 v22, v22, v45, v46\n"
    "v_max3_u32 v22, v22, v47, v108\n"
    "v_max3_u32 v22, v22, v109, v110\n"
    "ds_write_b128 v19, v[48:51] offset:24576\n"
    "v_max3_u32 v22, v22, v111, v48\n"
    "v_max3_u32 v22, v22, v49, v50\n"
    "ds_write_b128 v19, v[52:55] offset:25600\n"
    "v_max3_u32 v22, v22, v51, v52\n"
    "v_max3_u32 v22, v22, v53, v54\n"
    "ds_write_b128 v19, v[56:59] offset:26624\n"
    "v_max3_u32 v22, v22, v55, v56\n"
    "v_max3_u32 v22, v22, v57, v58\n"
    "v_max_u32_e32 v22, v22, v59\n"
    "v_cmp_eq_u32_e32 vcc, -1, v22\n"
    "s_nop 4\n"
    "s_cbranch_vccnz .Lrt3\n"
    ".Lrt3x:\n"
    "v_cndmask_b32_e64 v108, v108, 0, s[64:65]\n"
    "v_cndmask_b32_e64 v110, v110, 0, s[66:67]\n"
    "v_cndmask_b32_e64 v109, v109, 0, s[64:65]\n"
    "v_cndmask_b32_e64 v111, v111, 0, s[66:67]\n"
    "s_waitcnt lgkmcnt(0)\n"
    "s_barrier\n"
    "ds_read_b128 v[112:115], v78 offset:0\n"
    "ds_read_b128 v[116:119], v78 offset:1024\n"
    "ds_read_b128 v[120:123], v78 offset:2048\n"
    "ds_read_b128 v[124:127], v78 offset:3072\n"
    "ds_read_b128 v[128:131], v78 offset:4096\n"
    "ds_read_b128 v[132:135], v78 offset:5120\n"
    "ds_read_b128 v[136:139], v78 offset:6144\n"
    "ds_read_b128 v[140:143], v78 offset:7168\n"
    "s_waitcnt lgkmcnt(7)\n"
    "v_mfma_f32_16x16x32_bf16 v[36:39], v[112:115], v[108:111], v[28:31]\n"
    "ds_read_b128 v[112:115], v78 offset:8192\n"
    "s_add_u32 m0, s44, 122880\n"
    "s_nop 0\n"
    "buffer_load_dwordx4 v7, s[20:23], s46 offen offset:0  lds\n"
    "s_add_u32 s46, s46, 114688\n"
    "s_waitcnt lgkmcnt(7)\n"
    "v_mfma_f32_16x16x32_bf16 v[40:43], v[116:119], v[108:111], 0\n"
    "ds_read_b128 v[116:119], v78 offset:9216\n"
    "s_waitcnt lgkmcnt(7)\n"
    "v_mfma_f32_16x16x32_bf16 v[44:47], v[120:123], v[108:111], 0\n"
    "ds_read_b128 v[120:123], v78 offset:10240\n"
    "s_add_u32 m0, s44, 131072\n"
    "s_nop 0\n"
    "buffer_load_dwordx4 v6, s[16:19], s46 offen offset:0  lds\n"
    "s_waitcnt lgkmcnt(7)\n"
    "v_mfma_f32_16x16x32_bf16 v[48:51], v[124:127], v[108:111], 0\n"
    "ds_read_b128 v[124:127], v78 offset:11264\n"
    "s_waitcnt lgkmcnt(7)\n"
    "v_mfma_f32_16x16x32_bf16 v[52:55], v[128:131], v[108:111], 0\n"
    "ds_read_b128 v[128:131], v78 offset:12288\n"
    "v_max3_f32 v11, v36, v37, v38\n"
    "v_max_f32_e32 v11, v11, v39\n"
    "s_waitcnt lgkmcnt(7)\n"
    "v_mfma_f32_16x16x32_bf16 v[56:59], v[132:135], v[108:111], 0\n"
    "ds_read_b128 v[132:135], v78 offset:13312\n"
    "s_add_u32 m0, s44, 139264\n"
    "s_nop 0\n"
    "buffer_load_dwordx4 v7, s[20:23], s46 offen offset:0  lds\n"
    "s_add_u32 s46, s46, 114688\n"
    "v_max3_f32 v11, v11, v40, v41\n"
    "v_max3_f32 v11, v11, v42, v43\n"
    "s_waitcnt lgkmcnt(7)\n"
    "v_mfma_f32_16x16x32_bf16 v[60:63], v[136:139], v[108:111], 0\n"
    "ds_read_b128 v[136:139], v78 offset:14336\n"
    "v_max3_f32 v11, v11, v44, v45\n"
    "v_max3_f32 v11, v11, v46, v47\n"
    "s_waitcnt lgkmcnt(7)\n"
    "v_mfma_f32_16x16x32_bf16 v[64:67], v[140:143], v[108:111], 0\n"
    "ds_read_b128 v[140:143], v78 offset:15360\n"
    "v_max3_f32 v11, v11, v48, v49\n"
    "v_max3_f32 v11, v11, v50, v51\n"
    "s_waitcnt lgkmcnt(7)\n"
    "v_mfma_f32_16x16x32_bf16 v[68:71], v[112:115], v[108:111], 0\n"
    "ds_read_b128 v[112:115], v78 offset:16384\n"
    "s_add_u32 m0, s44, 147456\n"
    "s_nop 0\n"
    "buffer_load_dwordx4 v6, s[16:19], s46 offen offset:0  lds\n"
    "v_max3_f32 v11, v11, v52, v53\n"
    "v_max3_f32 v11, v11, v54, v55\n"
    "s_waitcnt lgkmcnt(7)\n"
    "v_mfma_f32_16x16x32_bf16 v[72:75], v[116:119], v[108:111], 0\n"
    "v_max3_f32 v11, v11, v56, v57\n"
    "v_max3_f32 v11, v11, v58, v59\n"
    "s_waitcnt lgkmcnt(6)\n"
    "v_mfma_f32_16x16x32_bf16 v[80:83], v[120:123], v[108:111], 0\n"
    "s_add_u32 m0, s44, 155648\n"
    "s_nop 0\n"
    "buffer_load_dwordx4 v7, s[20:23], s46 offen offset:0  lds\n"
    "s_add_u32 s46, s46, 114688\n"
    "v_max3_f32 v11, v11, v60, v61\n"
    "v_max3_f32 v11, v11, v62, v63\n"
    "s_waitcnt lgkmcnt(5)\n"
    "v_mfma_f32_16x16x32_bf16 v[84:87], v[124:127], v[108:111], 0\n"
    "v_max3_f32 v11, v11, v64, v65\n"
    "v_max3_f32 v11, v11, v66, v67\n"
    "s_waitcnt lgkmcnt(4)\n"
    "v_mfma_f32_16x16x32_bf16 v[88:91], v[128:131], v[108:111], 0\n"
    "v_max3_f32 v11, v11, v68, v69\n"
    "v_max3_f32 v11, v11, v70, v71\n"
    "s_waitcnt lgkmcnt(3)\n"
    "v_mfma_f32_16x16x32_bf16 v[92:95], v[132:135], v[108:111], 0\n"
    "buffer_load_dword v240, v18, s[24:27], s61 offen\n"
    "v_max3_f32 v11, v11, v72, v73\n"
    "v_max3_f32 v11, v11, v74, v75\n"
    "s_waitcnt lgkmcnt(2)\n"
    "v_mfma_f32_16x16x32_bf16 v[96:99], v[136:139], v[108:111], 0\n"
    "v_max3_f32 v11, v11, v80, v81\n"
    "v_max3_f32 v11, v11, v82, v83\n"
    "s_waitcnt lgkmcnt(1)\n"
    "v_mfma_f32_16x16x32_bf16 v[100:103], v[140:143], v[108:111], 0\n"
    "v_max3_f32 v11, v11, v84, v85\n"
    "v_max3_f32 v11, v11, v86, v87\n"
    "s_waitcnt lgkmcnt(0)\n"
    "v_mfma_f32_16x16x32_bf16 v[104:107], v[112:115], v[108:111], v[32:35]\n"
    "v_max3_f32 v11, v11, v88, v89\n"
    "v_max3_f32 v11, v11, v90, v91\n"
    "ds_read_b64_tr_b16 v[112:113], v77 offset:24576\n"
    "ds_read_b64_tr_b16 v[114:115], v77 offset:25600\n"
    "ds_read_b64_tr_b16 v[116:117], v77 offset:25088\n"
    "ds_read_b64_tr_b16 v[118:119], v77 offset:26112\n"
    "ds_read_b64_tr_b16 v[120:121], v77 offset:26624\n"
    "ds_read_b64_tr_b16 v[122:123], v77 offset:27648\n"
    "ds_read_b64_tr_b16 v[124:125], v77 offset:27136\n"
    "ds_read_b64_tr_b16 v[126:127], v77 offset:28160\n"
    "ds_read_b64_tr_b16 v[128:129], v77 offset:28672\n"
    "ds_read_b64_tr_b16 v[130:131], v77 offset:29696\n"
    "ds_read_b64_tr_b16 v[132:133], v77 offset:29184\n"
    "ds_read_b64_tr_b16 v[134:135], v77 offset:30208\n"
    "v_max3_f32 v11, v11, v92, v93\n"
    "v_max3_f32 v11, v11, v94, v95\n"
    "v_max3_f32 v11, v11, v96, v97\n"
    "v_max3_f32 v11, v11, v98, v99\n"
    "v_max3_f32 v11, v11, v100, v101\n"
    "v_max3_f32 v11, v11, v102, v103\n"
    "v_max3_f32 v11, v11, v104, v105\n"
    "v_max3_f32 v11, v11, v106, v107\n"
    "s_waitcnt vmcnt(7)\n"
    "s_barrier\n"
    "ds_read_b64_tr_b16 v[176:177], v9 offset:0\n"
    "ds_read_b64_tr_b16 v[178:179], v9 offset:4096\n"
    "ds_read_b64_tr_b16 v[180:181], v9 offset:2048\n"
    "ds_read_b64_tr_b16 v[182:183], v9 offset:6144\n"
    "ds_read_b64_tr_b16 v[184:185], v9 offset:16384\n"
    "ds_read_b64_tr_b16 v[186:187], v9 offset:20480\n"
    "ds_read_b64_tr_b16 v[188:189], v9 offset:18432\n"
    "ds_read_b64_tr_b16 v[190:191], v9 offset:22528\n"
    "s_cmp_ge_u32 s40, 2\n"
    "s_cbranch_scc1 .Lp2e\n"
    "s_getpc_b64 s[48:49]\n"
    ".Lpc1:\n"
    "s_lshl_b32 s50, s63, 4\n"
    "s_add_u32 s48, s48, s50\n"
    "s_addc_u32 s49, s49, 0\n"
    "s_add_u32 s48, s48, .Ltab-.Lpc1\n"
    "s_addc_u32 s49, s49, 0\n"
    "s_setpc_b64 s[48:49]\n"
    ".Ltab:\n"
    "v_mov_b32_e32 v100, s54\n"
    "v_mov_b32_e32 v101, s54\n"
    "v_mov_b32_e32 v102, s54\n"
    "v_mov_b32_e32 v103, s54\n"
    "v_mov_b32_e32 v96, s54\n"
    "v_mov_b32_e32 v97, s54\n"
    "v_mov_b32_e32 v98, s54\n"
    "v_mov_b32_e32 v99, s54\n"
    "v_mov_b32_e32 v92, s54\n"
    "v_mov_b32_e32 v93, s54\n"
    "v_mov_b32_e32 v94, s54\n"
    "v_mov_b32_e32 v95, s54\n"
    "v_mov_b32_e32 v88, s54\n"
    "v_mov_b32_e32 v89, s54\n"
    "v_mov_b32_e32 v90, s54\n"
    "v_mov_b32_e32 v91, s54\n"
    "v_mov_b32_e32 v84, s54\n"
    "v_mov_b32_e32 v85, s54\n"
    "v_mov_b32_e32 v86, s54\n"
    "v_mov_b32_e32 v87, s54\n"
    "v_mov_b32_e32 v80, s54\n"
    "v_mov_b32_e32 v81, s54\n"
    "v_mov_b32_e32 v82, s54\n"
    "v_mov_b32_e32 v83, s54\n"
    "v_mov_b32_e32 v72, s54\n"
    "v_mov_b32_e32 v73, s54\n"
    "v_mov_b32_e32 v74, s54\n"
    "v_mov_b32_e32 v75, s54\n"
    "v_mov_b32_e32 v68, s54\n"
    "v_mov_b32_e32 v69, s54\n"
    "v_mov_b32_e32 v70, s54\n"
    "v_mov_b32_e32 v71, s54\n"
    "v_mov_b32_e32 v64, s54\n"
    "v_mov_b32_e32 v65, s54\n"
    "v_mov_b32_e32 v66, s54\n"
    "v_mov_b32_e32 v67, s54\n"
    "v_mov_b32_e32 v60, s54\n"
    "v_mov_b32_e32 v61, s54\n"
    "v_mov_b32_e32 v62, s54\n"
    "v_mov_b32_e32 v63, s54\n"
    "v_mov_b32_e32 v56, s54\n"
    "v_mov_b32_e32 v57, s54\n"
    "v_mov_b32_e32 v58, s54\n"
    "v_mov_b32_e32 v59, s54\n"
    "v_mov_b32_e32 v52, s54\n"
    "v_mov_b32_e32 v53, s54\n"
    "v_mov_b32_e32 v54, s54\n"
    "v_mov_b32_e32 v55, s54\n"
    "v_mov_b32_e32 v48, s54\n"
    "v_mov_b32_e32 v49, s54\n"
    "v_mov_b32_e32 v50, s54\n"
    "v_mov_b32_e32 v51, s54\n"
    "v_mov_b32_e32 v44, s54\n"
    "v_mov_b32_e32 v45, s54\n"
    "v_mov_b32_e32 v46, s54\n"
    "v_mov_b32_e32 v47, s54\n"
    "v_mov_b32_e32 v40, s54\n"
    "v_mov_b32_e32 v41, s54\n"
    "v_mov_b32_e32 v42, s54\n"
    "v_mov_b32_e32 v43, s54\n"
    "v_mov_b32_e32 v36, s54\n"
    "v_mov_b32_e32 v37, s54\n"
    "v_mov_b32_e32 v38, s54\n"
    "v_mov_b32_e32 v39, s54\n"
    "v_max3_f32 v11, v36, v37, v38\n"
    "v_max_f32_e32 v11, v11, v39\n"
    "v_max3_f32 v11, v11, v40, v41\n"
    "v_max3_f32 v11, v11, v42, v43\n"
    "v_max3_f32 v11, v11, v44, v45\n"
    "v_max3_f32 v11, v11, v46, v47\n"
    "v_max3_f32 v11, v11, v48, v49\n"
    "v_max3_f32 v11, v11, v50, v51\n"
    "v_max3_f32 v11, v11, v52, v53\n"
    "v_max3_f32 v11, v11, v54, v55\n"
    "v_max3_f32 v11, v11, v56, v57\n"
    "v_max3_f32 v11, v11, v58, v59\n"
    "v_max3_f32 v11, v11, v60, v61\n"
    "v_max3_f32 v11, v11, v62, v63\n"
    "v_max3_f32 v11, v11, v64, v65\n"
    "v_max3_f32 v11, v11, v66, v67\n"
    "v_max3_f32 v11, v11, v68, v69\n"
    "v_max3_f32 v11, v11, v70, v71\n"
    "v_max3_f32 v11, v11, v72, v73\n"
    "v_max3_f32 v11, v11, v74, v75\n"
    "v_max3_f32 v11, v11, v80, v81\n"
    "v_max3_f32 v11, v11, v82, v83\n"
    "v_max3_f32 v11, v11, v84, v85\n"
    "v_max3_f32 v11, v11, v86, v87\n"
    "v_max3_f32 v11, v11, v88, v89\n"
    "v_max3_f32 v11, v11, v90, v91\n"
    "v_max3_f32 v11, v11, v92, v93\n"
    "v_max3_f32 v11, v11, v94, v95\n"
    "v_max3_f32 v11, v11, v96, v97\n"
    "v_max3_f32 v11, v11, v98, v99\n"
    "v_max3_f32 v11, v11, v100, v101\n"
    "v_max3_f32 v11, v11, v102, v103\n"
    "v_max3_f32 v11, v11, v104, v105\n"
    "v_max3_f32 v11, v11, v106, v107\n"
    ".Lp2e:\n"
    "v_mov_b32_e32 v16, v11\n"
    "s_nop 1\n"
    "v_permlane32_swap_b32_e32 v11, v16\n"
    "v_max_f32_e32 v11, v11, v16\n"
    "v_mov_b32_e32 v16, v11\n"
    "s_nop 1\n"
    "v_permlane16_swap_b32_e32 v11, v16\n"
    "v_max_f32_e32 v11, v11, v16\n"
    "v_mul_f32_e32 v11, 0xbe8b97f5, v11\n"
    "ds_read_b64_tr_b16 v[192:193], v9 offset:32768\n"
    "ds_read_b64_tr_b16 v[194:195], v9 offset:36864\n"
    "ds_read_b64_tr_b16 v[196:197], v9 offset:34816\n"
    "ds_read_b64_tr_b16 v[198:199], v9 offset:38912\n"
    "ds_read_b64_tr_b16 v[200:201], v8 offset:49152\n"
    "ds_read_b64_tr_b16 v[202:203], v8 offset:53248\n"
    "ds_read_b64_tr_b16 v[204:205], v8 offset:51200\n"
    "ds_read_b64_tr_b16 v[206:207], v8 offset:55296\n"
    "v_fmamk_f32 v36, v36, 0x3e8b97f5, v11\n"
    "v_fmamk_f32 v37, v37, 0x3e8b97f5, v11\n"
    "v_fmamk_f32 v38, v38, 0x3e8b97f5, v11\n"
    "v_fmamk_f32 v39, v39, 0x3e8b97f5, v11\n"
    "v_fmamk_f32 v40, v40, 0x3e8b97f5, v11\n"
    "v_fmamk_f32 v41, v41, 0x3e8b97f5, v11\n"
    "v_fmamk_f32 v42, v42, 0x3e8b97f5, v11\n"
    "v_fmamk_f32 v43, v43, 0x3e8b97f5, v11\n"
    "v_exp_f32_e32 v36, v36\n"
    "v_exp_f32_e32 v37, v37\n"
    "v_exp_f32_e32 v38, v38\n"
    "v_exp_f32_e32 v39, v39\n"
    "v_exp_f32_e32 v40, v40\n"
    "v_exp_f32_e32 v41, v41\n"
    "v_exp_f32_e32 v42, v42\n"
    "v_exp_f32_e32 v43, v43\n"
    "v_cvt_pk_bf16_f32 v232, v36, v37\n"
    "v_cvt_pk_bf16_f32 v233, v38, v39\n"
    "v_cvt_pk_bf16_f32 v234, v40, v41\n"
    "v_cvt_pk_bf16_f32 v235, v42, v43\n"
    "v_fmamk_f32 v44, v44, 0x3e8b97f5, v11\n"
    "v_fmamk_f32 v45, v45, 0x3e8b97f5, v11\n"
    "v_fmamk_f32 v46, v46, 0x3e8b97f5, v11\n"
    "v_fmamk_f32 v47, v47, 0x3e8b97f5, v11\n"
    "v_fmamk_f32 v48, v48, 0x3e8b97f5, v11\n"
    "v_fmamk_f32 v49, v49, 0x3e8b97f5, v11\n"
    "v_fmamk_f32 v50, v50, 0x3e8b97f5, v11\n"
    "v_fmamk_f32 v51, v51, 0x3e8b97f5, v11\n"
    "v_exp_f32_e32 v44, v44\n"
    "s_waitcnt lgkmcnt(0)\n"
    "s_waitcnt vmcnt(1)\n"
    "s_barrier\n"
    "v_mfma_f32_16x16x32_bf16 v[28:31], v[112:115], v[232:235], 0\n"
    "v_exp_f32_e32 v45, v45\n"
    "v_mfma_f32_16x16x32_bf16 v[32:35], v[116:119], v[232:235], 0\n"
    "v_exp_f32_e32 v46, v46\n"
    "v_mfma_f32_16x16x32_bf16 v[108:111], v[12:15], v[232:235], 0\n"
    "v_exp_f32_e32 v47, v47\n"
    "ds_read_b64_tr_b16 v[136:137], v77 offset:30720\n"
    "ds_read_b64_tr_b16 v[138:139], v77 offset:31744\n"
    "ds_read_b64_tr_b16 v[140:141], v77 offset:31232\n"
    "ds_read_b64_tr_b16 v[142:143], v77 offset:32256\n"
    "v_exp_f32_e32 v48, v48\n"
    "ds_read_b64_tr_b16 v[208:209], v9 offset:49152\n"
    "ds_read_b64_tr_b16 v[210:211], v9 offset:53248\n"
    "ds_read_b64_tr_b16 v[212:213], v9 offset:51200\n"
    "ds_read_b64_tr_b16 v[214:215], v9 offset:55296\n"
    "ds_read_b64_tr_b16 v[216:217], v10 offset:0\n"
    "ds_read_b64_tr_b16 v[218:219], v10 offset:4096\n"
    "v_exp_f32_e32 v49, v49\n"
    "ds_read_b64_tr_b16 v[220:221], v10 offset:2048\n"
    "ds_read_b64_tr_b16 v[222:223], v10 offset:6144\n"
    "ds_read_b64_tr_b16 v[224:225], v10 offset:16384\n"
    "ds_read_b64_tr_b16 v[226:227], v10 offset:20480\n"
    "ds_read_b64_tr_b16 v[228:229], v10 offset:18432\n"
    "ds_read_b64_tr_b16 v[230:231], v10 offset:22528\n"
    "v_exp_f32_e32 v50, v50\n"
    "s_add_u32 m0, s58, 0\n"
    "s_nop 0\n"
    "buffer_load_dwordx4 v79, s[12:15], s56 offen offset:0  lds\n"
    "s_add_u32 s56, s56, 7168\n"
    "v_exp_f32_e32 v51, v51\n"
    "s_add_u32 m0, s58, 1024\n"
    "s_nop 0\n"
    "buffer_load_dwordx4 v79, s[12:15], s56 offen offset:0  lds\n"
    "s_add_u32 s56, s56, 7168\n"
    "v_cvt_pk_bf16_f32 v236, v44, v45\n"
    "v_cvt_pk_bf16_f32 v237, v46, v47\n"
    "v_cvt_pk_bf16_f32 v238, v48, v49\n"
    "v_cvt_pk_bf16_f32 v239, v50, v51\n"
    "v_fmamk_f32 v52, v52, 0x3e8b97f5, v11\n"
    "v_fmamk_f32 v53, v53, 0x3e8b97f5, v11\n"
    "v_fmamk_f32 v54, v54, 0x3e8b97f5, v11\n"
    "v_fmamk_f32 v55, v55, 0x3e8b97f5, v11\n"
    "v_fmamk_f32 v56, v56, 0x3e8b97f5, v11\n"
    "v_fmamk_f32 v57, v57, 0x3e8b97f5, v11\n"
    "v_fmamk_f32 v58, v58, 0x3e8b97f5, v11\n"
    "v_fmamk_f32 v59, v59, 0x3e8b97f5, v11\n"
    "v_exp_f32_e32 v52, v52\n"
    "v_mfma_f32_16x16x32_bf16 v[28:31], v[120:123], v[236:239], v[28:31]\n"
    "v_exp_f32_e32 v53, v53\n"
    "v_mfma_f32_16x16x32_bf16 v[32:35], v[124:127], v[236:239], v[32:35]\n"
    "v_exp_f32_e32 v54, v54\n"
    "v_mfma_f32_16x16x32_bf16 v[108:111], v[12:15], v[236:239], v[108:111]\n"
    "v_exp_f32_e32 v55, v55\n"
    "v_exp_f32_e32 v56, v56\n"
    "ds_read_b64_tr_b16 v[112:113], v77 offset:32768\n"
    "ds_read_b64_tr_b16 v[114:115], v77 offset:33792\n"
    "ds_read_b64_tr_b16 v[116:117], v77 offset:33280\n"
    "ds_read_b64_tr_b16 v[118:119], v77 offset:34304\n"
    "v_exp_f32_e32 v57, v57\n"
    "s_add_u32 m0, s58, 2048\n"
    "s_nop 0\n"
    "buffer_load_dwordx4 v79, s[12:15], s56 offen offset:0  lds\n"
    "s_add_u32 s56, s56, 7168\n"
    "v_exp_f32_e32 v58, v58\n"
    "s_add_u32 m0, s58, 3072\n"
    "s_nop 0\n"
    "buffer_load_dwordx4 v79, s[12:15], s56 offen offset:0  lds\n"
    "s_add_u32 s56, s56, 7168\n"
    "v_exp_f32_e32 v59, v59\n"
    "v_cvt_pk_bf16_f32 v232, v52, v53\n"
    "v_cvt_pk_bf16_f32 v233, v54, v55\n"
    "v_cvt_pk_bf16_f32 v234, v56, v57\n"
    "v_cvt_pk_bf16_f32 v235, v58, v59\n"
    "v_fmamk_f32 v60, v60, 0x3e8b97f5, v11\n"
    "v_fmamk_f32 v61, v61, 0x3e8b97f5, v11\n"
    "v_fmamk_f32 v62, v62, 0x3e8b97f5, v11\n"
    "v_fmamk_f32 v63, v63, 0x3e8b97f5, v11\n"
    "v_fmamk_f32 v64, v64, 0x3e8b97f5, v11\n"
    "v_fmamk_f32 v65, v65, 0x3e8b97f5, v11\n"
    "v_fmamk_f32 v66, v66, 0x3e8b97f5, v11\n"
    "v_fmamk_f32 v67, v67, 0x3e8b97f5, v11\n"
    "v_exp_f32_e32 v60, v60\n"
    "v_mfma_f32_16x16x32_bf16 v[28:31], v[128:131], v[232:235], v[28:31]\n"
    "v_exp_f32_e32 v61, v61\n"
    "v_mfma_f32_16x16x32_bf16 v[32:35], v[132:135], v[232:235], v[32:35]\n"
    "v_exp_f32_e32 v62, v62\n"
    "v_exp_f32_e32 v63, v63\n"
    "v_mfma_f32_16x16x32_bf16 v[108:111], v[12:15], v[232:235], v[108:111]\n"
    "v_exp_f32_e32 v64, v64\n"
    "ds_read_b64_tr_b16 v[120:121], v77 offset:34816\n"
    "ds_read_b64_tr_b16 v[122:123], v77 offset:35840\n"
    "ds_read_b64_tr_b16 v[124:125], v77 offset:35328\n"
    "ds_read_b64_tr_b16 v[126:127], v77 offset:36352\n"
    "v_exp_f32_e32 v65, v65\n"
    "v_exp_f32_e32 v66, v66\n"
    "s_add_u32 m0, s58, 4096\n"
    "s_nop 0\n"
    "buffer_load_dwordx4 v79, s[12:15], s56 offen offset:0  lds\n"
    "s_add_u32 s56, s56, 7168\n"
    "v_exp_f32_e32 v67, v67\n"
    "v_cvt_pk_bf16_f32 v236, v60, v61\n"
    "v_cvt_pk_bf16_f32 v237, v62, v63\n"
    "v_cvt_pk_bf16_f32 v238, v64, v65\n"
    "v_cvt_pk_bf16_f32 v239, v66, v67\n"
    "v_fmamk_f32 v68, v68, 0x3e8b97f5, v11\n"
    "v_fmamk_f32 v69, v69, 0x3e8b97f5, v11\n"
    "v_fmamk_f32 v70, v70, 0x3e8b97f5, v11\n"
    "v_fmamk_f32 v71, v71, 0x3e8b97f5, v11\n"
    "v_fmamk_f32 v72, v72, 0x3e8b97f5, v11\n"
    "v_fmamk_f32 v73, v73, 0x3e8b97f5, v11\n"
    "v_fmamk_f32 v74, v74, 0x3e8b97f5, v11\n"
    "v_fmamk_f32 v75, v75, 0x3e8b97f5, v11\n"
    "v_exp_f32_e32 v68, v68\n"
    "s_waitcnt lgkmcnt(14)\n"
    "v_mfma_f32_16x16x32_bf16 v[28:31], v[136:139], v[236:239], v[28:31]\n"
    "v_exp_f32_e32 v69, v69\n"
    "v_mfma_f32_16x16x32_bf16 v[32:35], v[140:143], v[236:239], v[32:35]\n"
    "v_exp_f32_e32 v70, v70\n"
    "v_mfma_f32_16x16x32_bf16 v[108:111], v[12:15], v[236:239], v[108:111]\n"
    "v_exp_f32_e32 v71, v71\n"
    "ds_read_b64_tr_b16 v[128:129], v77 offset:36864\n"
    "ds_read_b64_tr_b16 v[130:131], v77 offset:37888\n"
    "ds_read_b64_tr_b16 v[132:133], v77 offset:37376\n"
    "ds_read_b64_tr_b16 v[134:135], v77 offset:38400\n"
    "v_exp_f32_e32 v72, v72\n"
    "s_waitcnt lgkmcnt(12)\n"
    "s_barrier\n"
    "v_exp_f32_e32 v73, v73\n"
    "s_add_u32 m0, s58, 5120\n"
    "s_nop 0\n"
    "buffer_load_dwordx4 v79, s[12:15], s56 offen offset:0  lds\n"
    "s_add_u32 s56, s56, 7168\n"
    "v_exp_f32_e32 v74, v74\n"
    "s_add_u32 m0, s58, 6144\n"
    "s_nop 0\n"
    "buffer_load_dwordx4 v79, s[12:15], s56 offen offset:0  lds\n"
    "s_add_u32 s56, s56, 7168\n"
    "v_exp_f32_e32 v75, v75\n"
    "v_cvt_pk_bf16_f32 v232, v68, v69\n"
    "v_cvt_pk_bf16_f32 v233, v70, v71\n"
    "v_cvt_pk_bf16_f32 v234, v72, v73\n"
    "v_cvt_pk_bf16_f32 v235, v74, v75\n"
    "v_fmamk_f32 v80, v80, 0x3e8b97f5, v11\n"
    "v_fmamk_f32 v81, v81, 0x3e8b97f5, v11\n"
    "v_fmamk_f32 v82, v82, 0x3e8b97f5, v11\n"
    "v_fmamk_f32 v83, v83, 0x3e8b97f5, v11\n"
    "v_fmamk_f32 v84, v84, 0x3e8b97f5, v11\n"
    "v_fmamk_f32 v85, v85, 0x3e8b97f5, v11\n"
    "v_fmamk_f32 v86, v86, 0x3e8b97f5, v11\n"
    "v_fmamk_f32 v87, v87, 0x3e8b97f5, v11\n"
    "v_exp_f32_e32 v80, v80\n"
    "s_waitcnt lgkmcnt(10)\n"
    "v_mfma_f32_16x16x32_bf16 v[28:31], v[112:115], v[232:235], v[28:31]\n"
    "v_exp_f32_e32 v81, v81\n"
    "s_waitcnt lgkmcnt(8)\n"
    "v_mfma_f32_16x16x32_bf16 v[32:35], v[116:119], v[232:235], v[32:35]\n"
    "v_exp_f32_e32 v82, v82\n"
    "v_mfma_f32_16x16x32_bf16 v[108:111], v[12:15], v[232:235], v[108:111]\n"
    "v_exp_f32_e32 v83, v83\n"
    "ds_read_b64_tr_b16 v[136:137], v77 offset:38912\n"
    "ds_read_b64_tr_b16 v[138:139], v77 offset:39936\n"
    "ds_read_b64_tr_b16 v[140:141], v77 offset:39424\n"
    "ds_read_b64_tr_b16 v[142:143], v77 offset:40448\n"
    "v_exp_f32_e32 v84, v84\n"
    "s_add_u32 m0, s44, 106496\n"
    "s_nop 0\n"
    "buffer_load_dwordx4 v76, s[4:7], s57 offen offset:0  lds\n"
    "v_exp_f32_e32 v85, v85\n"
    "s_add_u32 m0, s44, 114688\n"
    "s_nop 0\n"
    "buffer_load_dwordx4 v6, s[16:19], s46 offen offset:0  lds\n"
    "v_exp_f32_e32 v86, v86\n"
    "s_add_u32 m0, s44, 122880\n"
    "s_nop 0\n"
    "buffer_load_dwordx4 v7, s[20:23], s46 offen offset:0  lds\n"
    "s_add_u32 s46, s46, 114688\n"
    "v_exp_f32_e32 v87, v87\n"
    "v_cvt_pk_bf16_f32 v236, v80, v81\n"
    "v_cvt_pk_bf16_f32 v237, v82, v83\n"
    "v_cvt_pk_bf16_f32 v238, v84, v85\n"
    "v_cvt_pk_bf16_f32 v239, v86, v87\n"
    "v_fmamk_f32 v88, v88, 0x3e8b97f5, v11\n"
    "v_fmamk_f32 v89, v89, 0x3e8b97f5, v11\n"
    "v_fmamk_f32 v90, v90, 0x3e8b97f5, v11\n"
    "v_fmamk_f32 v91, v91, 0x3e8b97f5, v11\n"
    "v_fmamk_f32 v92, v92, 0x3e8b97f5, v11\n"
    "v_fmamk_f32 v93, v93, 0x3e8b97f5, v11\n"
    "v_fmamk_f32 v94, v94, 0x3e8b97f5, v11\n"
    "v_fmamk_f32 v95, v95, 0x3e8b97f5, v11\n"
    "v_exp_f32_e32 v88, v88\n"
    "s_waitcnt lgkmcnt(10)\n"
    "v_mfma_f32_16x16x32_bf16 v[28:31], v[120:123], v[236:239], v[28:31]\n"
    "v_exp_f32_e32 v89, v89\n"
    "s_waitcnt lgkmcnt(8)\n"
    "v_mfma_f32_16x16x32_bf16 v[32:35], v[124:127], v[236:239], v[32:35]\n"
    "v_exp_f32_e32 v90, v90\n"
    "v_exp_f32_e32 v91, v91\n"
    "v_mfma_f32_16x16x32_bf16 v[108:111], v[12:15], v[236:239], v[108:111]\n"
    "v_exp_f32_e32 v92, v92\n"
    "ds_read_b64_tr_b16 v[112:113], v77 offset:40960\n"
    "ds_read_b64_tr_b16 v[114:115], v77 offset:40960\n"
    "ds_read_b64_tr_b16 v[116:117], v77 offset:41472\n"
    "ds_read_b64_tr_b16 v[118:119], v77 offset:41472\n"
    "v_exp_f32_e32 v93, v93\n"
    "v_exp_f32_e32 v94, v94\n"
    "s_add_u32 m0, s44, 131072\n"
    "s_nop 0\n"
    "buffer_load_dwordx4 v6, s[16:19], s46 offen offset:0  lds\n"
    "v_exp_f32_e32 v95, v95\n"
    "v_cvt_pk_bf16_f32 v232, v88, v89\n"
    "v_cvt_pk_bf16_f32 v233, v90, v91\n"
    "v_cvt_pk_bf16_f32 v234, v92, v93\n"
    "v_cvt_pk_bf16_f32 v235, v94, v95\n"
    "v_fmamk_f32 v96, v96, 0x3e8b97f5, v11\n"
    "v_fmamk_f32 v97, v97, 0x3e8b97f5, v11\n"
    "v_fmamk_f32 v98, v98, 0x3e8b97f5, v11\n"
    "v_fmamk_f32 v99, v99, 0x3e8b97f5, v11\n"
    "v_fmamk_f32 v100, v100, 0x3e8b97f5, v11\n"
    "v_fmamk_f32 v101, v101, 0x3e8b97f5, v11\n"
    "v_fmamk_f32 v102, v102, 0x3e8b97f5, v11\n"
    "v_fmamk_f32 v103, v103, 0x3e8b97f5, v11\n"
    "v_exp_f32_e32 v96, v96\n"
    "s_waitcnt lgkmcnt(10)\n"
    "v_mfma_f32_16x16x32_bf16 v[28:31], v[128:131], v[232:235], v[28:31]\n"
    "v_exp_f32_e32 v97, v97\n"
    "s_waitcnt lgkmcnt(8)\n"
    "v_mfma_f32_16x16x32_bf16 v[32:35], v[132:135], v[232:235], v[32:35]\n"
    "v_exp_f32_e32 v98, v98\n"
    "v_mfma_f32_16x16x32_bf16 v[108:111], v[12:15], v[232:235], v[108:111]\n"
    "v_exp_f32_e32 v99, v99\n"
    "v_exp_f32_e32 v100, v100\n"
    "s_add_u32 m0, s44, 139264\n"
    "s_nop 0\n"
    "buffer_load_dwordx4 v7, s[20:23], s46 offen offset:0  lds\n"
    "s_add_u32 s46, s46, 114688\n"
    "v_exp_f32_e32 v101, v101\n"
    "s_add_u32 m0, s44, 147456\n"
    "s_nop 0\n"
    "buffer_load_dwordx4 v6, s[16:19], s46 offen offset:0  lds\n"
    "v_exp_f32_e32 v102, v102\n"
    "s_lshl_b32 s52, s38, 2\n"
    "s_and_b32 s53, s3, 3\n"
    "s_add_u32 s52, s52, s53\n"
    "s_lshl1_add_u32 s52, s52, s42\n"
    "s_lshr_b32 s53, s3, 2\n"
    "s_lshl1_add_u32 s52, s52, s53\n"
    "s_lshl_b32 s52, s52, 6\n"
    "s_add_u32 s52, s52, 36864\n"
    "s_lshl_b32 s53, s38, 10\n"
    "s_add_u32 s53, s53, 4992\n"
    "v_and_b32_e32 v36, 7, v0\n"
    "v_lshl_add_u32 v36, v36, 3, s52\n"
    "v_lshl_add_u32 v37, v2, 3, s53\n"
    "v_cmp_gt_u32_e32 vcc, 32, v0\n"
    "s_nop 1\n"
    "v_cndmask_b32_e32 v38, v37, v36, vcc\n"
    "v_exp_f32_e32 v103, v103\n"
    "v_cvt_pk_bf16_f32 v236, v96, v97\n"
    "v_cvt_pk_bf16_f32 v237, v98, v99\n"
    "v_cvt_pk_bf16_f32 v238, v100, v101\n"
    "v_cvt_pk_bf16_f32 v239, v102, v103\n"
    "v_fmamk_f32 v104, v104, 0x3e8b97f5, v11\n"
    "v_fmamk_f32 v105, v105, 0x3e8b97f5, v11\n"
    "v_fmamk_f32 v106, v106, 0x3e8b97f5, v11\n"
    "v_fmamk_f32 v107, v107, 0x3e8b97f5, v11\n"
    "v_exp_f32_e32 v104, v104\n"
    "s_waitcnt lgkmcnt(6)\n"
    "v_mfma_f32_16x16x32_bf16 v[28:31], v[136:139], v[236:239], v[28:31]\n"
    "s_waitcnt lgkmcnt(4)\n"
    "v_mfma_f32_16x16x32_bf16 v[32:35], v[140:143], v[236:239], v[32:35]\n"
    "v_exp_f32_e32 v105, v105\n"
    "v_mfma_f32_16x16x32_bf16 v[108:111], v[12:15], v[236:239], v[108:111]\n"
    "v_exp_f32_e32 v106, v106\n"
    "s_add_u32 m0, s44, 155648\n"
    "s_nop 0\n"
    "buffer_load_dwordx4 v7, s[20:23], s46 offen offset:0  lds\n"
    "s_add_u32 s46, s46, 114688\n"
    "v_exp_f32_e32 v107, v107\n"
    "buffer_load_dwordx2 v[36:37], v38, s[28:31], 0 offen sc1\n"
    "v_cvt_pk_bf16_f32 v232, v104, v105\n"
    "v_cvt_pk_bf16_f32 v233, v106, v107\n"
    "v_mov_b32_e32 v234, 0\n"
    "v_mov_b32_e32 v235, 0\n"
    "s_nop 1\n"
    "v_mfma_f32_16x16x32_bf16 v[108:111], v[12:15], v[232:235], v[108:111]\n"
    "s_waitcnt lgkmcnt(2)\n"
    "v_mfma_f32_16x16x32_bf16 v[28:31], v[112:115], v[232:235], v[28:31]\n"
    "s_waitcnt lgkmcnt(0)\n"
    "v_mfma_f32_16x16x32_bf16 v[32:35], v[116:119], v[232:235], v[32:35]\n"
    "s_nop 3\n"
    "v_rcp_f32_e32 v108, v108\n"
    "s_nop 0\n"
    "v_cndmask_b32_e64 v110, v108, 0, s[66:67]\n"
    "v_cndmask_b32_e64 v108, v108, 0, s[64:65]\n"
    "v_pk_mul_f32 v[28:29], v[28:29], v[108:109] op_sel_hi:[1,0]\n"
    "v_pk_mul_f32 v[30:31], v[30:31], v[108:109] op_sel_hi:[1,0]\n"
    "v_pk_mul_f32 v[32:33], v[32:33], v[110:111] op_sel_hi:[1,0]\n"
    "v_pk_mul_f32 v[34:35], v[34:35], v[110:111] op_sel_hi:[1,0]\n"
    "v_cvt_pk_bf16_f32 v20, v28, v29\n"
    "v_cvt_pk_bf16_f32 v21, v30, v31\n"
    "v_cvt_pk_bf16_f32 v22, v32, v33\n"
    "v_cvt_pk_bf16_f32 v23, v34, v35\n"
    "v_min_u32_e32 v20, -2, v20\n"
    "v_min_u32_e32 v21, -2, v21\n"
    "v_min_u32_e32 v22, -2, v22\n"
    "v_min_u32_e32 v23, -2, v23\n"
    "buffer_store_dwordx4 v[20:23], v1, s[28:31], s59 offen sc1\n"
    "s_cmp_ge_u32 s2, 224\n"
    "s_cbranch_scc1 .Lhelper1\n"
    "s_waitcnt vmcnt(2)\n"
    "s_barrier\n"
    "s_add_u32 m0, s45, 0\n"
    "s_nop 0\n"
    "buffer_load_dwordx4 v79, s[24:27], s47 offen offset:0  lds\n"
    "s_add_u32 s47, s47, 7168\n"
    "ds_read_b64_tr_b16 v[232:233], v9 offset:49152\n"
    "ds_read_b64_tr_b16 v[234:235], v9 offset:53248\n"
    "ds_read_b64_tr_b16 v[236:237], v9 offset:51200\n"
    "s_and_b32 s56, s3, 3\n"
    "s_lshr_b32 s57, s3, 2\n"
    "s_lshl_b32 s52, s38, 2\n"
    "s_add_u32 s52, s52, s56\n"
    "s_lshl_b32 s53, s52, 1\n"
    "s_add_u32 s53, s53, s42\n"
    "s_lshl_b32 s53, s53, 1\n"
    "s_add_u32 s53, s53, s57\n"
    "s_lshl_b32 s58, s53, 13\n"
    "s_add_u32 s58, s58, 7340032\n"
    "s_add_u32 s62, s58, 4096\n"
    "s_lshl_b32 s53, s43, 1\n"
    "s_add_u32 s53, s53, s57\n"
    "s_lshl_b32 s54, s38, 17\n"
    "s_add_u32 s54, s54, 9437184\n"
    "s_mul_i32 s48, s56, 14\n"
    "s_add_u32 s48, s48, s53\n"
    "s_lshl_b32 s48, s48, 3\n"
    "s_add_u32 s48, s48, s42\n"
    "s_add_u32 m0, s45, 1024\n"
    "s_nop 0\n"
    "buffer_load_dwordx4 v79, s[24:27], s47 offen offset:0  lds\n"
    "s_add_u32 s47, s47, 7168\n"
    "ds_read_b64_tr_b16 v[238:239], v9 offset:55296\n"
    "ds_read_b64_tr_b16 v[240:241], v10 offset:0\n"
    "ds_read_b64_tr_b16 v[242:243], v10 offset:4096\n"
    "s_lshl_b32 s48, s48, 8\n"
    "s_add_u32 s48, s48, s54\n"
    "s_lshl_b32 s49, s53, 2\n"
    "s_add_u32 s49, s49, s56\n"
    "s_lshl_b32 s49, s49, 1\n"
    "s_add_u32 s49, s49, s42\n"
    "s_lshl_b32 s49, s49, 7\n"
    "s_add_u32 s49, s49, 114688\n"
    "s_add_u32 s49, s49, s54\n"
    "s_add_u32 s50, s101, 16384\n"
    "s_lshl_b32 s63, s3, 12\n"
    "v_and_b32_e32 v74, 48, v1\n"
    "v_lshrrev_b32_e32 v11, 2, v0\n"
    "v_lshrrev_b32_e32 v17, 1, v74\n"
    "v_lshl_add_u32 v11, v11, 7, v17\n"
    "s_lshl_b32 s52, s57, 1\n"
    "v_bfe_u32 v16, v0, 3, 2\n"
    "v_xor_b32_e32 v16, s52, v16\n"
    "s_add_u32 m0, s45, 2048\n"
    "s_nop 0\n"
    "buffer_load_dwordx4 v79, s[24:27], s47 offen offset:0  lds\n"
    "s_add_u32 s47, s47, 7168\n"
    "ds_read_b64_tr_b16 v[244:245], v10 offset:2048\n"
    "ds_read_b64_tr_b16 v[246:247], v10 offset:6144\n"
    "ds_read_b64_tr_b16 v[248:249], v10 offset:16384\n"
    "v_lshrrev_b32_e32 v17, 5, v0\n"
    "s_mul_i32 s52, s57, 28672\n"
    "s_add_u32 s52, s52, 49152\n"
    "v_xor_b32_e32 v18, 0, v16\n"
    "v_lshl_add_u32 v18, v18, 5, v11\n"
    "v_add_u32_e32 v19, s52, v18\n"
    "v_lshl_add_u32 v78, v17, 10, v18\n"
    "v_add_u32_e32 v78, s45, v78\n"
    "v_xor_b32_e32 v18, 1, v16\n"
    "v_lshl_add_u32 v18, v18, 5, v11\n"
    "v_add_u32_e32 v26, s52, v18\n"
    "v_lshl_add_u32 v77, v17, 10, v18\n"
    "v_add_u32_e32 v77, s45, v77\n"
    "v_xor_b32_e32 v18, 2, v16\n"
    "v_lshl_add_u32 v18, v18, 5, v11\n"
    "v_add_u32_e32 v27, s52, v18\n"
    "v_lshl_add_u32 v76, v17, 10, v18\n"
    "v_add_u32_e32 v76, s45, v76\n"
    "v_xor_b32_e32 v18, 3, v16\n"
    "s_add_u32 m0, s45, 3072\n"
    "s_nop 0\n"
    "buffer_load_dwordx4 v79, s[24:27], s47 offen offset:0  lds\n"
    "s_add_u32 s47, s47, 7168\n"
    "ds_read_b64_tr_b16 v[250:251], v10 offset:20480\n"
    "ds_read_b64_tr_b16 v[252:253], v10 offset:18432\n"
    "ds_read_b64_tr_b16 v[254:255], v10 offset:22528\n"
    "v_lshl_add_u32 v18, v18, 5, v11\n"
    "v_add_u32_e32 v28, s52, v18\n"
    "v_lshl_add_u32 v75, v17, 10, v18\n"
    "v_add_u32_e32 v75, s45, v75\n"
    "s_lshl_b32 s52, s57, 2\n"
    "v_add_u32_e32 v11, s52, v17\n"
    "v_lshrrev_b32_e32 v16, 1, v2\n"
    "v_xor_b32_e32 v11, v11, v16\n"
    "v_lshlrev_b32_e32 v29, 4, v11\n"
    "v_lshl_add_u32 v29, v2, 7, v29\n"
    "v_and_b32_e32 v11, 1, v3\n"
    "v_lshl_add_u32 v29, v11, 3, v29\n"
    "s_lshl_b32 s52, s56, 11\n"
    "s_add_u32 s52, s52, 106496\n"
    "v_add_u32_e32 v29, s52, v29\n"
    "v_lshlrev_b32_e32 v30, 10, v17\n"
    "v_lshl_add_u32 v30, v11, 9, v30\n"
    "v_lshl_add_u32 v30, v2, 4, v30\n"
    "ds_read_b64 v[32:33], v29 offset:0\n"
    "v_xor_b32_e32 v29, 32, v29\n"
    "ds_read_b64 v[34:35], v29 offset:0\n"
    "v_dot2c_f32_bf16_e32 v24, v12, v176\n"
    "v_dot2c_f32_bf16_e32 v25, v12, v177\n"
    "v_dot2c_f32_bf16_e32 v24, v12, v178\n"
    "v_dot2c_f32_bf16_e32 v25, v12, v179\n"
    "v_dot2c_f32_bf16_e32 v24, v12, v180\n"
    "v_dot2c_f32_bf16_e32 v25, v12, v181\n"
    "v_dot2c_f32_bf16_e32 v24, v12, v182\n"
    "v_dot2c_f32_bf16_e32 v25, v12, v183\n"
    "v_dot2c_f32_bf16_e32 v24, v12, v184\n"
    "v_dot2c_f32_bf16_e32 v25, v12, v185\n"
    "v_dot2c_f32_bf16_e32 v24, v12, v186\n"
    "v_dot2c_f32_bf16_e32 v25, v12, v187\n"
    "v_dot2c_f32_bf16_e32 v24, v12, v188\n"
    "v_dot2c_f32_bf16_e32 v25, v12, v189\n"
    "v_dot2c_f32_bf16_e32 v24, v12, v190\n"
    "v_dot2c_f32_bf16_e32 v25, v12, v191\n"
    "v_dot2c_f32_bf16_e32 v24, v12, v192\n"
    "v_dot2c_f32_bf16_e32 v25, v12, v193\n"
    "v_dot2c_f32_bf16_e32 v24, v12, v194\n"
    "v_dot2c_f32_bf16_e32 v25, v12, v195\n"
    "v_dot2c_f32_bf16_e32 v24, v12, v196\n"
    "v_dot2c_f32_bf16_e32 v25, v12, v197\n"
    "v_dot2c_f32_bf16_e32 v24, v12, v198\n"
    "v_dot2c_f32_bf16_e32 v25, v12, v199\n"
    "v_dot2c_f32_bf16_e32 v24, v12, v200\n"
    "v_dot2c_f32_bf16_e32 v25, v12, v201\n"
    "v_dot2c_f32_bf16_e32 v24, v12, v202\n"
    "v_dot2c_f32_bf16_e32 v25, v12, v203\n"
    "v_dot2c_f32_bf16_e32 v24, v12, v204\n"
    "v_dot2c_f32_bf16_e32 v25, v12, v205\n"
    "v_dot2c_f32_bf16_e32 v24, v12, v206\n"
    "v_dot2c_f32_bf16_e32 v25, v12, v207\n"
    "s_waitcnt vmcnt(5)\n"
    "v_cmp_ne_u64_e32 vcc, s[36:37], v[36:37]\n"
    "s_nop 4\n"
    "s_cbranch_vccz .Lrt4x\n"
    ".Lrt4:\n"
    "s_cmp_eq_u32 s100, 0\n"
    "s_cbranch_scc1 .Lrt4x\n"
    "s_sub_u32 s100, s100, 1\n"
    "s_mov_b64 exec, vcc\n"
    "buffer_load_dwordx2 v[36:37], v38, s[28:31], 0 offen sc1\n"
    "s_mov_b64 exec, -1\n"
    "s_waitcnt vmcnt(0)\n"
    "v_cmp_ne_u64_e32 vcc, s[36:37], v[36:37]\n"
    "s_nop 4\n"
    "s_cbranch_vccnz .Lrt4\n"
    ".Lrt4x:\n"
    "s_sleep 1\n"
    "buffer_load_dwordx4 v[96:99], v1, s[28:31], s58 offen offset:0 sc1\n"
    "buffer_load_dwordx4 v[100:103], v1, s[28:31], s58 offen offset:1024 sc1\n"
    "buffer_load_dwordx4 v[104:107], v1, s[28:31], s58 offen offset:2048 sc1\n"
    "buffer_load_dwordx4 v[108:111], v1, s[28:31], s58 offen offset:3072 sc1\n"
    "buffer_load_dwordx4 v[112:115], v1, s[28:31], s62 offen offset:0 sc1\n"
    "buffer_load_dwordx4 v[116:119], v1, s[28:31], s62 offen offset:1024 sc1\n"
    "buffer_load_dwordx4 v[120:123], v1, s[28:31], s62 offen offset:2048 sc1\n"
    "buffer_load_dwordx4 v[124:127], v1, s[28:31], s62 offen offset:3072 sc1\n"
    "v_dot2c_f32_bf16_e32 v24, v12, v208\n"
    "v_dot2c_f32_bf16_e32 v25, v12, v209\n"
    "v_dot2c_f32_bf16_e32 v24, v12, v210\n"
    "v_dot2c_f32_bf16_e32 v25, v12, v211\n"
    "v_dot2c_f32_bf16_e32 v24, v12, v212\n"
    "v_dot2c_f32_bf16_e32 v25, v12, v213\n"
    "v_dot2c_f32_bf16_e32 v24, v12, v214\n"
    "v_dot2c_f32_bf16_e32 v25, v12, v215\n"
    "v_dot2c_f32_bf16_e32 v24, v12, v216\n"
    "v_dot2c_f32_bf16_e32 v25, v12, v217\n"
    "v_dot2c_f32_bf16_e32 v24, v12, v218\n"
    "v_dot2c_f32_bf16_e32 v25, v12, v219\n"
    "v_dot2c_f32_bf16_e32 v24, v12, v220\n"
    "v_dot2c_f32_bf16_e32 v25, v12, v221\n"
    "v_dot2c_f32_bf16_e32 v24, v12, v222\n"
    "v_dot2c_f32_bf16_e32 v25, v12, v223\n"
    "v_dot2c_f32_bf16_e32 v24, v12, v224\n"
    "v_dot2c_f32_bf16_e32 v25, v12, v225\n"
    "v_dot2c_f32_bf16_e32 v24, v12, v226\n"
    "v_dot2c_f32_bf16_e32 v25, v12, v227\n"
    "v_dot2c_f32_bf16_e32 v24, v12, v228\n"
    "v_dot2c_f32_bf16_e32 v25, v12, v229\n"
    "v_dot2c_f32_bf16_e32 v24, v12, v230\n"
    "v_dot2c_f32_bf16_e32 v25, v12, v231\n"
    "s_waitcnt lgkmcnt(13)\n"
    "v_dot2c_f32_bf16_e32 v24, v12, v232\n"
    "v_dot2c_f32_bf16_e32 v25, v12, v233\n"
    "s_waitcnt lgkmcnt(12)\n"
    "v_dot2c_f32_bf16_e32 v24, v12, v234\n"
    "v_dot2c_f32_bf16_e32 v25, v12, v235\n"
    "s_waitcnt lgkmcnt(11)\n"
    "v_dot2c_f32_bf16_e32 v24, v12, v236\n"
    "v_dot2c_f32_bf16_e32 v25, v12, v237\n"
    "s_waitcnt lgkmcnt(10)\n"
    "v_dot2c_f32_bf16_e32 v24, v12, v238\n"
    "v_dot2c_f32_bf16_e32 v25, v12, v239\n"
    "s_waitcnt lgkmcnt(9)\n"
    "v_dot2c_f32_bf16_e32 v24, v12, v240\n"
    "v_dot2c_f32_bf16_e32 v25, v12, v241\n"
    "s_waitcnt lgkmcnt(8)\n"
    "v_dot2c_f32_bf16_e32 v24, v12, v242\n"
    "v_dot2c_f32_bf16_e32 v25, v12, v243\n"
    "s_waitcnt lgkmcnt(7)\n"
    "v_dot2c_f32_bf16_e32 v24, v12, v244\n"
    "v_dot2c_f32_bf16_e32 v25, v12, v245\n"
    "s_waitcnt lgkmcnt(6)\n"
    "v_dot2c_f32_bf16_e32 v24, v12, v246\n"
    "v_dot2c_f32_bf16_e32 v25, v12, v247\n"
    "s_waitcnt lgkmcnt(5)\n"
    "v_dot2c_f32_bf16_e32 v24, v12, v248\n"
    "v_dot2c_f32_bf16_e32 v25, v12, v249\n"
    "s_waitcnt lgkmcnt(4)\n"
    "v_dot2c_f32_bf16_e32 v24, v12, v250\n"
    "v_dot2c_f32_bf16_e32 v25, v12, v251\n"
    "s_waitcnt lgkmcnt(3)\n"
    "v_dot2c_f32_bf16_e32 v24, v12, v252\n"
    "v_dot2c_f32_bf16_e32 v25, v12, v253\n"
    "s_waitcnt lgkmcnt(2)\n"
    "v_dot2c_f32_bf16_e32 v24, v12, v254\n"
    "v_dot2c_f32_bf16_e32 v25, v12, v255\n"
    "s_waitcnt lgkmcnt(0)\n"
    "s_barrier\n"
    "s_add_u32 s51, s45, 114688\n"
    "s_add_u32 m0, s51, 0\n"
    "s_nop 0\n"
    "buffer_load_dwordx4 v79, s[24:27], s47 offen offset:0  lds\n"
    "s_add_u32 s47, s47, 7168\n"
    "s_add_u32 m0, s51, 1024\n"
    "s_nop 0\n"
    "buffer_load_dwordx4 v79, s[24:27], s47 offen offset:0  lds\n"
    "s_add_u32 s47, s47, 7168\n"
    "s_add_u32 m0, s51, 2048\n"
    "s_nop 0\n"
    "buffer_load_dwordx4 v79, s[24:27], s47 offen offset:0  lds\n"
    "s_add_u32 s47, s47, 7168\n"
    "s_add_u32 m0, s51, 3072\n"
    "s_nop 0\n"
    "buffer_load_dwordx4 v79, s[24:27], s47 offen offset:0  lds\n"
    "s_add_u32 s47, s47, 7168\n"
    "v_add_u32_e32 v74, s45, v1\n"
    "s_xor_b32 s52, s3, 4\n"
    "s_mul_i32 s52, s52, 6144\n"
    "v_add_u32_e32 v10, s52, v1\n"
    ".Lredo3:\n"
    "ds_read_b64_tr_b16 v[128:129], v27 offset:0\n"
    "ds_read_b64_tr_b16 v[130:131], v27 offset:2048\n"
    "ds_read_b64_tr_b16 v[132:133], v28 offset:0\n"
    "ds_read_b64_tr_b16 v[134:135], v28 offset:2048\n"
    "ds_read_b64_tr_b16 v[136:137], v19 offset:0\n"
    "ds_read_b64_tr_b16 v[138:139], v19 offset:2048\n"
    "ds_read_b64_tr_b16 v[140:141], v26 offset:0\n"
    "ds_read_b64_tr_b16 v[142:143], v26 offset:2048\n"
    "ds_read_b64_tr_b16 v[48:49], v27 offset:3072\n"
    "ds_read_b64_tr_b16 v[50:51], v27 offset:5120\n"
    "ds_read_b64_tr_b16 v[52:53], v28 offset:3072\n"
    "ds_read_b64_tr_b16 v[54:55], v28 offset:5120\n"
    "ds_read_b64_tr_b16 v[56:57], v19 offset:3072\n"
    "ds_read_b64_tr_b16 v[58:59], v19 offset:5120\n"
    "v_lshlrev_b32_e32 v6, 16, v32\n"
    "v_and_b32_e32 v7, 0xffff0000, v32\n"
    "v_lshlrev_b32_e32 v8, 16, v33\n"
    "v_and_b32_e32 v9, 0xffff0000, v33\n"
    "v_lshlrev_b32_e32 v36, 16, v34\n"
    "v_and_b32_e32 v37, 0xffff0000, v34\n"
    "v_lshlrev_b32_e32 v38, 16, v35\n"
    "v_and_b32_e32 v39, 0xffff0000, v35\n"
    "s_waitcnt vmcnt(11) lgkmcnt(12)\n"
    "v_mfma_f32_16x16x32_bf16 v[40:43], v[128:131], v[96:99], 0\n"
    "ds_read_b64_tr_b16 v[60:61], v26 offset:3072\n"
    "ds_read_b64_tr_b16 v[62:63], v26 offset:5120\n"
    "v_max3_u32 v17, v96, v97, v98\n"
    "s_waitcnt lgkmcnt(12)\n"
    "v_mfma_f32_16x16x32_bf16 v[44:47], v[132:135], v[96:99], 0\n"
    "ds_read_b64_tr_b16 v[128:129], v27 offset:7168\n"
    "ds_read_b64_tr_b16 v[130:131], v27 offset:9216\n"
    "s_waitcnt lgkmcnt(12)\n"
    "v_mfma_f32_16x16x32_bf16 v[6:9], v[136:139], v[96:99], v[6:9]\n"
    "ds_read_b64_tr_b16 v[132:133], v28 offset:7168\n"
    "ds_read_b64_tr_b16 v[134:135], v28 offset:9216\n"
    "s_waitcnt lgkmcnt(12)\n"
    "v_mfma_f32_16x16x32_bf16 v[36:39], v[140:143], v[96:99], v[36:39]\n"
    "ds_read_b64_tr_b16 v[136:137], v19 offset:7168\n"
    "ds_read_b64_tr_b16 v[138:139], v19 offset:9216\n"
    "s_waitcnt vmcnt(10) lgkmcnt(12)\n"
    "v_mfma_f32_16x16x32_bf16 v[40:43], v[48:51], v[100:103], v[40:43]\n"
    "ds_read_b64_tr_b16 v[140:141], v26 offset:7168\n"
    "ds_read_b64_tr_b16 v[142:143], v26 offset:9216\n"
    "v_max3_u32 v17, v17, v99, v100\n"
    "v_max3_u32 v17, v17, v101, v102\n"
    "s_waitcnt lgkmcnt(12)\n"
    "v_mfma_f32_16x16x32_bf16 v[44:47], v[52:55], v[100:103], v[44:47]\n"
    "ds_read_b64_tr_b16 v[48:49], v27 offset:10240\n"
    "ds_read_b64_tr_b16 v[50:51], v27 offset:12288\n"
    "s_waitcnt lgkmcnt(12)\n"
    "v_mfma_f32_16x16x32_bf16 v[6:9], v[56:59], v[100:103], v[6:9]\n"
    "ds_read_b64_tr_b16 v[52:53], v28 offset:10240\n"
    "ds_read_b64_tr_b16 v[54:55], v28 offset:12288\n"
    "s_waitcnt lgkmcnt(12)\n"
    "v_mfma_f32_16x16x32_bf16 v[36:39], v[60:63], v[100:103], v[36:39]\n"
    "ds_read_b64_tr_b16 v[56:57], v19 offset:10240\n"
    "ds_read_b64_tr_b16 v[58:59], v19 offset:12288\n"
    "s_waitcnt vmcnt(9) lgkmcnt(12)\n"
    "v_mfma_f32_16x16x32_bf16 v[40:43], v[128:131], v[104:107], v[40:43]\n"
    "ds_read_b64_tr_b16 v[60:61], v26 offset:10240\n"
    "ds_read_b64_tr_b16 v[62:63], v26 offset:12288\n"
    "v_max3_u32 v17, v17, v103, v104\n"
    "v_max3_u32 v17, v17, v105, v106\n"
    "s_waitcnt lgkmcnt(12)\n"
    "v_mfma_f32_16x16x32_bf16 v[44:47], v[132:135], v[104:107], v[44:47]\n"
    "ds_read_b64_tr_b16 v[128:129], v27 offset:14336\n"
    "ds_read_b64_tr_b16 v[130:131], v27 offset:16384\n"
    "s_waitcnt lgkmcnt(12)\n"
    "v_mfma_f32_16x16x32_bf16 v[6:9], v[136:139], v[104:107], v[6:9]\n"
    "ds_read_b64_tr_b16 v[132:133], v28 offset:14336\n"
    "ds_read_b64_tr_b16 v[134:135], v28 offset:16384\n"
    "s_waitcnt lgkmcnt(12)\n"
    "v_mfma_f32_16x16x32_bf16 v[36:39], v[140:143], v[104:107], v[36:39]\n"
    "ds_read_b64_tr_b16 v[136:137], v19 offset:14336\n"
    "ds_read_b64_tr_b16 v[138:139], v19 offset:16384\n"
    "s_waitcnt vmcnt(8) lgkmcnt(12)\n"
    "v_mfma_f32_16x16x32_bf16 v[40:43], v[48:51], v[108:111], v[40:43]\n"
    "ds_read_b64_tr_b16 v[140:141], v26 offset:14336\n"
    "ds_read_b64_tr_b16 v[142:143], v26 offset:16384\n"
    "v_max3_u32 v17, v17, v107, v108\n"
    "v_max3_u32 v17, v17, v109, v110\n"
    "s_waitcnt lgkmcnt(12)\n"
    "v_mfma_f32_16x16x32_bf16 v[44:47], v[52:55], v[108:111], v[44:47]\n"
    "ds_read_b64_tr_b16 v[48:49], v27 offset:17408\n"
    "ds_read_b64_tr_b16 v[50:51], v27 offset:19456\n"
    "s_waitcnt lgkmcnt(12)\n"
    "v_mfma_f32_16x16x32_bf16 v[6:9], v[56:59], v[108:111], v[6:9]\n"
    "ds_read_b64_tr_b16 v[52:53], v28 offset:17408\n"
    "ds_read_b64_tr_b16 v[54:55], v28 offset:19456\n"
    "s_waitcnt lgkmcnt(12)\n"
    "v_mfma_f32_16x16x32_bf16 v[36:39], v[60:63], v[108:111], v[36:39]\n"
    "ds_read_b64_tr_b16 v[56:57], v19 offset:17408\n"
    "ds_read_b64_tr_b16 v[58:59], v19 offset:19456\n"
    "s_waitcnt vmcnt(7) lgkmcnt(12)\n"
    "v_mfma_f32_16x16x32_bf16 v[40:43], v[128:131], v[112:115], v[40:43]\n"
    "ds_read_b64_tr_b16 v[60:61], v26 offset:17408\n"
    "ds_read_b64_tr_b16 v[62:63], v26 offset:19456\n"
    "v_max3_u32 v17, v17, v111, v112\n"
    "v_max3_u32 v17, v17, v113, v114\n"
    "s_waitcnt lgkmcnt(12)\n"
    "v_mfma_f32_16x16x32_bf16 v[44:47], v[132:135], v[112:115], v[44:47]\n"
    "ds_read_b64_tr_b16 v[128:129], v27 offset:21504\n"
    "ds_read_b64_tr_b16 v[130:131], v27 offset:23552\n"
    "s_waitcnt lgkmcnt(12)\n"
    "v_mfma_f32_16x16x32_bf16 v[6:9], v[136:139], v[112:115], v[6:9]\n"
    "ds_read_b64_tr_b16 v[132:133], v28 offset:21504\n"
    "ds_read_b64_tr_b16 v[134:135], v28 offset:23552\n"
    "s_waitcnt lgkmcnt(12)\n"
    "v_mfma_f32_16x16x32_bf16 v[36:39], v[140:143], v[112:115], v[36:39]\n"
    "ds_read_b64_tr_b16 v[136:137], v19 offset:21504\n"
    "ds_read_b64_tr_b16 v[138:139], v19 offset:23552\n"
    "s_waitcnt vmcnt(6) lgkmcnt(12)\n"
    "v_mfma_f32_16x16x32_bf16 v[40:43], v[48:51], v[116:119], v[40:43]\n"
    "ds_read_b64_tr_b16 v[140:141], v26 offset:21504\n"
    "ds_read_b64_tr_b16 v[142:143], v26 offset:23552\n"
    "v_max3_u32 v17, v17, v115, v116\n"
    "v_max3_u32 v17, v17, v117, v118\n"
    "s_waitcnt lgkmcnt(12)\n"
    "v_mfma_f32_16x16x32_bf16 v[44:47], v[52:55], v[116:119], v[44:47]\n"
    "ds_read_b64_tr_b16 v[48:49], v27 offset:24576\n"
    "ds_read_b64_tr_b16 v[50:51], v27 offset:26624\n"
    "s_waitcnt lgkmcnt(12)\n"
    "v_mfma_f32_16x16x32_bf16 v[6:9], v[56:59], v[116:119], v[6:9]\n"
    "ds_read_b64_tr_b16 v[52:53], v28 offset:24576\n"
    "ds_read_b64_tr_b16 v[54:55], v28 offset:26624\n"
    "s_waitcnt lgkmcnt(12)\n"
    "v_mfma_f32_16x16x32_bf16 v[36:39], v[60:63], v[116:119], v[36:39]\n"
    "ds_read_b64_tr_b16 v[56:57], v19 offset:24576\n"
    "ds_read_b64_tr_b16 v[58:59], v19 offset:26624\n"
    "s_waitcnt vmcnt(5) lgkmcnt(12)\n"
    "v_mfma_f32_16x16x32_bf16 v[40:43], v[128:131], v[120:123], v[40:43]\n"
    "ds_read_b64_tr_b16 v[60:61], v26 offset:24576\n"
    "ds_read_b64_tr_b16 v[62:63], v26 offset:26624\n"
    "v_max3_u32 v17, v17, v119, v120\n"
    "v_max3_u32 v17, v17, v121, v122\n"
    "s_waitcnt lgkmcnt(12)\n"
    "v_mfma_f32_16x16x32_bf16 v[44:47], v[132:135], v[120:123], v[44:47]\n"
    "s_waitcnt lgkmcnt(10)\n"
    "v_mfma_f32_16x16x32_bf16 v[6:9], v[136:139], v[120:123], v[6:9]\n"
    "s_waitcnt lgkmcnt(8)\n"
    "v_mfma_f32_16x16x32_bf16 v[36:39], v[140:143], v[120:123], v[36:39]\n"
    "s_waitcnt vmcnt(4) lgkmcnt(6)\n"
    "v_mfma_f32_16x16x32_bf16 v[40:43], v[48:51], v[124:127], v[40:43]\n"
    "v_max3_u32 v17, v17, v123, v124\n"
    "v_max3_u32 v17, v17, v125, v126\n"
    "v_max_u32_e32 v17, v17, v127\n"
    "v_cmp_eq_u32_e32 vcc, -1, v17\n"
    "s_waitcnt lgkmcnt(4)\n"
    "v_mfma_f32_16x16x32_bf16 v[44:47], v[52:55], v[124:127], v[44:47]\n"
    "s_waitcnt lgkmcnt(2)\n"
    "v_mfma_f32_16x16x32_bf16 v[6:9], v[56:59], v[124:127], v[6:9]\n"
    "s_waitcnt lgkmcnt(0)\n"
    "v_mfma_f32_16x16x32_bf16 v[36:39], v[60:63], v[124:127], v[36:39]\n"
    "s_cbranch_vccz .Lrt5x\n"
    ".Lrt5:\n"
    "s_cmp_eq_u32 s100, 0\n"
    "s_cbranch_scc1 .Lrt5x\n"
    "s_sub_u32 s100, s100, 1\n"
    "s_mov_b64 exec, vcc\n"
    "buffer_load_dwordx4 v[96:99], v1, s[28:31], s58 offen offset:0 sc1\n"
    "buffer_load_dwordx4 v[100:103], v1, s[28:31], s58 offen offset:1024 sc1\n"
    "buffer_load_dwordx4 v[104:107], v1, s[28:31], s58 offen offset:2048 sc1\n"
    "buffer_load_dwordx4 v[108:111], v1, s[28:31], s58 offen offset:3072 sc1\n"
    "buffer_load_dwordx4 v[112:115], v1, s[28:31], s62 offen offset:0 sc1\n"
    "buffer_load_dwordx4 v[116:119], v1, s[28:31], s62 offen offset:1024 sc1\n"
    "buffer_load_dwordx4 v[120:123], v1, s[28:31], s62 offen offset:2048 sc1\n"
    "buffer_load_dwordx4 v[124:127], v1, s[28:31], s62 offen offset:3072 sc1\n"
    "s_mov_b64 exec, -1\n"
    "s_waitcnt vmcnt(0)\n"
    "v_max3_u32 v17, v96, v97, v98\n"
    "v_max3_u32 v17, v17, v99, v100\n"
    "v_max3_u32 v17, v17, v101, v102\n"
    "v_max3_u32 v17, v17, v103, v104\n"
    "v_max3_u32 v17, v17, v105, v106\n"
    "v_max3_u32 v17, v17, v107, v108\n"
    "v_max3_u32 v17, v17, v109, v110\n"
    "v_max3_u32 v17, v17, v111, v112\n"
    "v_max3_u32 v17, v17, v113, v114\n"
    "v_max3_u32 v17, v17, v115, v116\n"
    "v_max3_u32 v17, v17, v117, v118\n"
    "v_max3_u32 v17, v17, v119, v120\n"
    "v_max3_u32 v17, v17, v121, v122\n"
    "v_max3_u32 v17, v17, v123, v124\n"
    "v_max3_u32 v17, v17, v125, v126\n"
    "v_max_u32_e32 v17, v17, v127\n"
    "v_cmp_eq_u32_e32 vcc, -1, v17\n"
    "s_nop 4\n"
    "s_cbranch_vccnz .Lrt5\n"
    "s_nop 15\n"
    "s_branch .Lredo3\n"
    ".Lrt5x:\n"
    "ds_write_b128 v74, v[40:43] offset:4096\n"
    "s_nop 1\n"
    "ds_write_b128 v74, v[44:47] offset:5120\n"
    "ds_read_b64_tr_b16 v[80:81], v78 offset:0\n"
    "ds_read_b64_tr_b16 v[82:83], v78 offset:1024\n"
    "ds_read_b64_tr_b16 v[84:85], v77 offset:0\n"
    "ds_read_b64_tr_b16 v[86:87], v77 offset:1024\n"
    "ds_read_b64_tr_b16 v[88:89], v76 offset:0\n"
    "ds_read_b64_tr_b16 v[90:91], v76 offset:1024\n"
    "ds_read_b64_tr_b16 v[92:93], v75 offset:0\n"
    "ds_read_b64_tr_b16 v[94:95], v75 offset:1024\n"
    "s_waitcnt lgkmcnt(0)\n"
    "s_barrier\n"
    "ds_read_b128 v[40:43], v10 offset:4096\n"
    "ds_read_b128 v[44:47], v10 offset:5120\n"
    "v_mov_b32_e32 v11, 0\n"
    "v_mov_b32_e32 v16, 0\n"
    "v_lshlrev_b32_e32 v18, 3, v2\n"
    "v_add_u32_e32 v78, 114688, v78\n"
    "v_add_u32_e32 v77, 114688, v77\n"
    "v_add_u32_e32 v76, 114688, v76\n"
    "v_add_u32_e32 v75, 114688, v75\n"
    "s_waitcnt lgkmcnt(1)\n"
    "v_pk_add_f32 v[6:7], v[6:7], v[40:41]\n"
    "v_pk_add_f32 v[8:9], v[8:9], v[42:43]\n"
    "v_cvt_pk_bf16_f32 v70, v6, v7\n"
    "v_cvt_pk_bf16_f32 v71, v8, v9\n"
    "s_waitcnt lgkmcnt(0)\n"
    "v_pk_add_f32 v[36:37], v[36:37], v[44:45]\n"
    "v_pk_add_f32 v[38:39], v[38:39], v[46:47]\n"
    "v_cvt_pk_bf16_f32 v72, v36, v37\n"
    "v_cvt_pk_bf16_f32 v73, v38, v39\n"
    "v_min_u32_e32 v70, -2, v70\n"
    "v_min_u32_e32 v71, -2, v71\n"
    "v_min_u32_e32 v72, -2, v72\n"
    "v_min_u32_e32 v73, -2, v73\n"
    "buffer_store_dwordx4 v[70:73], v30, s[28:31], s48 offen sc1\n"
    "v_mov_b32_e32 v20, 0\n"
    "v_mov_b32_e32 v21, 0\n"
    "v_dot2c_f32_bf16_e32 v11, v12, v70\n"
    "v_dot2c_f32_bf16_e32 v16, v70, v70\n"
    "v_dot2c_f32_bf16_e32 v20, v12, v71\n"
    "v_dot2c_f32_bf16_e32 v21, v71, v71\n"
    "v_dot2c_f32_bf16_e32 v11, v12, v72\n"
    "v_dot2c_f32_bf16_e32 v16, v72, v72\n"
    "v_dot2c_f32_bf16_e32 v20, v12, v73\n"
    "v_dot2c_f32_bf16_e32 v21, v73, v73\n"
    "s_nop 1\n"
    "v_add_f32_e32 v11, v11, v20\n"
    "v_add_f32_e32 v16, v16, v21\n"
    "s_nop 1\n"
    "v_permlane32_swap_b32_e32 v11, v16\n"
    "v_add_f32_e32 v11, v11, v16\n"
    "v_mov_b32_e32 v16, v11\n"
    "s_nop 1\n"
    "v_permlane16_swap_b32_e32 v11, v16\n"
    "v_add_f32_e32 v20, v11, v16\n"
    "v_min_u32_e32 v20, -2, v20\n"
    "v_mov_b32_e32 v21, v20\n"
    "v_mov_b32_e32 v11, v20\n"
    "s_nop 1\n"
    "v_permlane32_swap_b32_e32 v11, v21\n"
    "s_mov_b64 exec, 0xffff\n"
    "buffer_store_dwordx2 v[20:21], v18, s[28:31], s49 offen sc1\n"
    "s_mov_b64 exec, -1\n"
    "s_waitcnt vmcnt(2)\n"
    "ds_read_b64_tr_b16 v[96:97], v78 offset:0\n"
    "ds_read_b64_tr_b16 v[98:99], v78 offset:1024\n"
    "ds_read_b64_tr_b16 v[100:101], v77 offset:0\n"
    "ds_read_b64_tr_b16 v[102:103], v77 offset:1024\n"
    "ds_read_b64_tr_b16 v[104:105], v76 offset:0\n"
    "ds_read_b64_tr_b16 v[106:107], v76 offset:1024\n"
    "ds_read_b64_tr_b16 v[108:109], v75 offset:0\n"
    "ds_read_b64_tr_b16 v[110:111], v75 offset:1024\n"
    "s_add_u32 m0, s63, 61440\n"
    "s_nop 0\n"
    "buffer_load_dwordx4 v79, s[24:27], s47 offen offset:0  lds\n"
    "s_add_u32 s47, s47, 7168\n"
    "s_lshl_b32 s48, s38, 17\n"
    "s_add_u32 s48, s48, 9437184\n"
    "s_add_u32 s6, s48, s44\n"
    "v_add_u32_e32 v11, s44, v1\n"
    "s_add_u32 m0, s63, 62464\n"
    "s_nop 0\n"
    "buffer_load_dwordx4 v79, s[24:27], s47 offen offset:0  lds\n"
    "s_add_u32 s47, s47, 7168\n"
    "s_lshl_b32 s55, s3, 7\n"
    "s_add_u32 s7, s48, s55\n"
    "s_add_u32 s7, s7, 114688\n"
    "s_add_u32 m0, s63, 63488\n"
    "s_nop 0\n"
    "buffer_load_dwordx4 v79, s[24:27], s47 offen offset:0  lds\n"
    "s_add_u32 s47, s47, 7168\n"
    "v_lshlrev_b32_e32 v16, 3, v2\n"
    "v_lshl_add_u32 v16, v3, 10, v16\n"
    "v_mov_b32_e32 v118, 0\n"
    "v_mov_b32_e32 v119, 0\n"
    "s_add_u32 m0, s63, 64512\n"
    "s_nop 0\n"
    "buffer_load_dwordx4 v79, s[24:27], s47 offen offset:0  lds\n"
    "s_add_u32 s47, s47, 7168\n"
    "s_min_u32 s10, s63, 24576\n"
    "s_add_u32 s9, s48, s10\n"
    "s_add_u32 s9, s9, 57344\n"
    "s_waitcnt lgkmcnt(0)\n"
    "s_barrier\n"
    "s_sleep 4\n"
    "buffer_load_dwordx4 v[26:29], v1, s[28:31], s6 offen offset:0 sc1\n"
    "s_add_u32 s22, s6, 8192\n"
    "buffer_load_dwordx4 v[30:33], v1, s[28:31], s22 offen offset:0 sc1\n"
    "s_add_u32 s22, s6, 16384\n"
    "buffer_load_dwordx4 v[34:37], v1, s[28:31], s22 offen offset:0 sc1\n"
    "s_add_u32 s22, s6, 24576\n"
    "buffer_load_dwordx4 v[38:41], v1, s[28:31], s22 offen offset:0 sc1\n"
    "s_lshl_b32 s52, s39, 3\n"
    "s_add_u32 s52, s52, s3\n"
    "s_mul_i32 s11, s38, 448\n"
    "s_add_u32 s11, s11, s52\n"
    "s_lshl_b32 s11, s11, 10\n"
    "s_add_u32 s11, s11, 11534336\n"
    "v_and_b32_e32 v19, 0x1f0, v1\n"
    "v_add_u32_e32 v19, 159744, v19\n"
    "s_lshl_b32 s52, s3, 7\n"
    "s_add_u32 s52, s52, 161792\n"
    "v_lshrrev_b32_e32 v17, 5, v0\n"
    "v_lshl_add_u32 v62, v17, 4, s52\n"
    "v_and_b32_e32 v18, 31, v0\n"
    "v_lshl_add_u32 v18, v18, 2, s52\n"
    "v_add_f32_e32 v24, v24, v25\n"
    "v_mov_b32_e32 v25, v24\n"
    "s_nop 1\n"
    "v_permlane32_swap_b32_e32 v24, v25\n"
    "v_add_f32_e32 v24, v24, v25\n"
    "ds_write_b32 v18, v24\n"
    "s_lshl_b32 s12, s38, 10\n"
    "s_add_u32 s12, s12, 20480\n"
    "s_lshl_b32 s52, s3, 11\n"
    "s_sub_u32 s52, 4294914048, s52\n"
    "v_add_u32_e32 v78, s52, v78\n"
    "v_add_u32_e32 v77, s52, v77\n"
    "v_add_u32_e32 v76, s52, v76\n"
    "v_add_u32_e32 v75, s52, v75\n"
    "v_add_u32_e32 v10, 126976, v1\n"
    "s_mul_i32 s16, s38, 458752\n"
    "s_mul_i32 s52, s3, 14336\n"
    "s_add_u32 s16, s16, s52\n"
    "s_lshl_b32 s52, s42, 8\n"
    "s_add_u32 s16, s16, s52\n"
    "s_add_u32 s16, s16, 11534336\n"
    "s_lshl_b32 s52, s38, 2\n"
    "s_add_u32 s52, s52, s57\n"
    "s_lshl_b32 s52, s52, 1\n"
    "s_add_u32 s52, s52, s42\n"
    "s_lshl_b32 s52, s52, 2\n"
    "s_add_u32 s52, s52, s56\n"
    "s_mul_i32 s17, s52, 3584\n"
    "s_lshl_b32 s52, s43, 7\n"
    "s_add_u32 s17, s17, s52\n"
    "s_lshl_b32 s52, s57, 3\n"
    "v_xor_b32_e32 v18, s52, v3\n"
    "s_add_u32 s52, s63, 28672\n"
    "v_xor_b32_e32 v63, 0, v18\n"
    "v_xor_b32_e32 v17, v63, v2\n"
    "v_lshl_add_u32 v17, v17, 4, s52\n"
    "v_lshl_add_u32 v63, v63, 8, v17\n"
    "v_xor_b32_e32 v64, 4, v18\n"
    "v_xor_b32_e32 v17, v64, v2\n"
    "v_lshl_add_u32 v17, v17, 4, s52\n"
    "v_lshl_add_u32 v64, v64, 8, v17\n"
    "v_xor_b32_e32 v65, 8, v18\n"
    "v_xor_b32_e32 v17, v65, v2\n"
    "v_lshl_add_u32 v17, v17, 4, s52\n"
    "v_lshl_add_u32 v65, v65, 8, v17\n"
    "v_xor_b32_e32 v66, 12, v18\n"
    "v_xor_b32_e32 v17, v66, v2\n"
    "v_lshl_add_u32 v17, v17, 4, s52\n"
    "v_lshl_add_u32 v66, v66, 8, v17\n"
    "s_waitcnt vmcnt(8)\n"
    "s_mov_b64 exec, 1\n"
    "buffer_store_dwordx2 v[4:5], off, s[28:31], s50 sc1\n"
    "s_mov_b64 exec, -1\n"
    "s_add_u32 s22, s6, 32768\n"
    "buffer_load_dwordx4 v[128:131], v1, s[28:31], s22 offen offset:0 sc1\n"
    "s_add_u32 s22, s6, 40960\n"
    "buffer_load_dwordx4 v[132:135], v1, s[28:31], s22 offen offset:0 sc1\n"
    "s_add_u32 s22, s6, 49152\n"
    "buffer_load_dwordx4 v[136:139], v1, s[28:31], s22 offen offset:0 sc1\n"
    "buffer_load_dwordx2 v[112:113], v16, s[28:31], s7 offen sc1\n"
    "s_add_u32 s20, s7, 4096\n"
    "buffer_load_dwordx2 v[114:115], v16, s[28:31], s20 offen sc1\n"
    "s_add_u32 s20, s7, 8192\n"
    "buffer_load_dwordx2 v[116:117], v16, s[28:31], s20 offen sc1\n"
    "s_add_u32 s20, s7, 12288\n"
    "s_mov_b32 exec_hi, 0\n"
    "buffer_load_dwordx2 v[118:119], v16, s[28:31], s20 offen sc1\n"
    "s_mov_b64 exec, -1\n"
    "s_waitcnt vmcnt(11)\n"
    "ds_write_b128 v11, v[26:29] offset:0\n"
    "v_max3_u32 v17, v26, v27, v28\n"
    "s_waitcnt vmcnt(10)\n"
    "ds_write_b128 v11, v[30:33] offset:8192\n"
    "v_max3_u32 v17, v17, v29, v30\n"
    "v_max3_u32 v17, v17, v31, v32\n"
    "s_waitcnt vmcnt(9)\n"
    "ds_write_b128 v11, v[34:37] offset:16384\n"
    "v_max3_u32 v17, v17, v33, v34\n"
    "v_max3_u32 v17, v17, v35, v36\n"
    "s_waitcnt vmcnt(8)\n"
    "ds_write_b128 v11, v[38:41] offset:24576\n"
    "v_max3_u32 v17, v17, v37, v38\n"
    "v_max3_u32 v17, v17, v39, v40\n"
    "v_max_u32_e32 v17, v17, v41\n"
    "v_cmp_eq_u32_e32 vcc, -1, v17\n"
    "s_nop 2\n"
    "s_cbranch_vccz .Lrt6x\n"
    ".Lrt6:\n"
    "s_cmp_eq_u32 s100, 0\n"
    "s_cbranch_scc1 .Lrt6x\n"
    "s_sub_u32 s100, s100, 1\n"
    "s_mov_b64 exec, vcc\n"
    "buffer_load_dwordx4 v[26:29], v1, s[28:31], s6 offen offset:0 sc1\n"
    "s_add_u32 s22, s6, 8192\n"
    "buffer_load_dwordx4 v[30:33], v1, s[28:31], s22 offen offset:0 sc1\n"
    "s_add_u32 s22, s6, 16384\n"
    "buffer_load_dwordx4 v[34:37], v1, s[28:31], s22 offen offset:0 sc1\n"
    "s_add_u32 s22, s6, 24576\n"
    "buffer_load_dwordx4 v[38:41], v1, s[28:31], s22 offen offset:0 sc1\n"
    "s_mov_b64 exec, -1\n"
    "s_waitcnt vmcnt(0)\n"
    "ds_write_b128 v11, v[26:29] offset:0\n"
    "v_max3_u32 v17, v26, v27, v28\n"
    "ds_write_b128 v11, v[30:33] offset:8192\n"
    "v_max3_u32 v17, v17, v29, v30\n"
    "v_max3_u32 v17, v17, v31, v32\n"
    "ds_write_b128 v11, v[34:37] offset:16384\n"
    "v_max3_u32 v17, v17, v33, v34\n"
    "v_max3_u32 v17, v17, v35, v36\n"
    "ds_write_b128 v11, v[38:41] offset:24576\n"
    "v_max3_u32 v17, v17, v37, v38\n"
    "v_max3_u32 v17, v17, v39, v40\n"
    "v_max_u32_e32 v17, v17, v41\n"
    "v_cmp_eq_u32_e32 vcc, -1, v17\n"
    "s_nop 4\n"
    "s_cbranch_vccnz .Lrt6\n"
    ".Lrt6x:\n"
    "s_waitcnt lgkmcnt(0)\n"
    "s_barrier\n"
    "ds_read_b128 v[6:9], v1 offset:0\n"
    "ds_read_b128 v[12:15], v1 offset:1024\n"
    "ds_read_b128 v[42:45], v1 offset:2048\n"
    "ds_read_b128 v[46:49], v1 offset:3072\n"
    "ds_read_b128 v[50:53], v1 offset:4096\n"
    "s_waitcnt lgkmcnt(4)\n"
    "v_mfma_f32_32x32x16_bf16 v[26:41], v[144:147], v[6:9], 0\n"
    "ds_read_b128 v[6:9], v1 offset:5120\n"
    "s_waitcnt lgkmcnt(4)\n"
    "v_mfma_f32_32x32x16_bf16 v[26:41], v[148:151], v[12:15], v[26:41]\n"
    "ds_read_b128 v[12:15], v1 offset:6144\n"
    "s_waitcnt lgkmcnt(4)\n"
    "v_mfma_f32_32x32x16_bf16 v[26:41], v[152:155], v[42:45], v[26:41]\n"
    "ds_read_b128 v[42:45], v1 offset:7168\n"
    "s_waitcnt vmcnt(6)\n"
    "ds_write_b128 v11, v[128:131] offset:32768\n"
    "v_max3_u32 v17, v128, v129, v130\n"
    "s_waitcnt vmcnt(5)\n"
    "ds_write_b128 v11, v[132:135] offset:40960\n"
    "v_max3_u32 v17, v17, v131, v132\n"
    "s_waitcnt lgkmcnt(6)\n"
    "v_mfma_f32_32x32x16_bf16 v[26:41], v[156:159], v[46:49], v[26:41]\n"
    "ds_read_b128 v[46:49], v1 offset:8192\n"
    "v_max3_u32 v17, v17, v133, v134\n"
    "s_waitcnt vmcnt(4)\n"
    "ds_write_b128 v11, v[136:139] offset:49152\n"
    "v_max3_u32 v17, v17, v135, v136\n"
    "s_waitcnt lgkmcnt(7)\n"
    "v_mfma_f32_32x32x16_bf16 v[26:41], v[160:163], v[50:53], v[26:41]\n"
    "ds_read_b128 v[50:53], v1 offset:9216\n"
    "v_max3_u32 v17, v17, v137, v138\n"
    "v_max_u32_e32 v17, v17, v139\n"
    "v_cmp_eq_u32_e32 vcc, -1, v17\n"
    "s_waitcnt lgkmcnt(7)\n"
    "v_mfma_f32_32x32x16_bf16 v[26:41], v[164:167], v[6:9], v[26:41]\n"
    "ds_read_b128 v[6:9], v1 offset:10240\n"
    "s_waitcnt lgkmcnt(7)\n"
    "v_mfma_f32_32x32x16_bf16 v[26:41], v[168:171], v[12:15], v[26:41]\n"
    "ds_read_b128 v[12:15], v1 offset:11264\n"
    "s_cbranch_vccz .Lrt7x\n"
    ".Lrt7:\n"
    "s_cmp_eq_u32 s100, 0\n"
    "s_cbranch_scc1 .Lrt7x\n"
    "s_sub_u32 s100, s100, 1\n"
    "s_mov_b64 exec, vcc\n"
    "s_add_u32 s22, s6, 32768\n"
    "buffer_load_dwordx4 v[128:131], v1, s[28:31], s22 offen offset:0 sc1\n"
    "s_add_u32 s22, s6, 40960\n"
    "buffer_load_dwordx4 v[132:135], v1, s[28:31], s22 offen offset:0 sc1\n"
    "s_add_u32 s22, s6, 49152\n"
    "buffer_load_dwordx4 v[136:139], v1, s[28:31], s22 offen offset:0 sc1\n"
    "s_mov_b64 exec, -1\n"
    "s_waitcnt vmcnt(0)\n"
    "ds_write_b128 v11, v[128:131] offset:32768\n"
    "v_max3_u32 v17, v128, v129, v130\n"
    "ds_write_b128 v11, v[132:135] offset:40960\n"
    "v_max3_u32 v17, v17, v131, v132\n"
    "v_max3_u32 v17, v17, v133, v134\n"
    "ds_write_b128 v11, v[136:139] offset:49152\n"
    "v_max3_u32 v17, v17, v135, v136\n"
    "v_max3_u32 v17, v17, v137, v138\n"
    "v_max_u32_e32 v17, v17, v139\n"
    "v_cmp_eq_u32_e32 vcc, -1, v17\n"
    "s_nop 4\n"
    "s_cbranch_vccnz .Lrt7\n"
    ".Lrt7x:\n"
    "s_waitcnt lgkmcnt(7)\n"
    "v_mfma_f32_32x32x16_bf16 v[26:41], v[172:175], v[42:45], v[26:41]\n"
    "ds_read_b128 v[42:45], v1 offset:12288\n"
    "s_waitcnt vmcnt(2)\n"
    "v_max3_u32 v17, v112, v113, v114\n"
    "s_waitcnt vmcnt(1)\n"
    "v_max3_u32 v17, v17, v115, v116\n"
    "s_waitcnt vmcnt(0)\n"
    "v_max3_u32 v17, v17, v117, v118\n"
    "s_waitcnt lgkmcnt(5)\n"
    "v_mfma_f32_32x32x16_bf16 v[26:41], v[176:179], v[46:49], v[26:41]\n"
    "ds_read_b128 v[46:49], v1 offset:13312\n"
    "v_max_u32_e32 v17, v17, v119\n"
    "v_cmp_eq_u32_e32 vcc, -1, v17\n"
    "s_add_u32 m0, s63, 94208\n"
    "s_add_u32 s21, s47, 0\n"
    "buffer_load_dwordx4 v79, s[24:27], s21 offen offset:0  lds\n"
    "s_waitcnt lgkmcnt(4)\n"
    "v_mfma_f32_32x32x16_bf16 v[26:41], v[180:183], v[50:53], v[26:41]\n"
    "ds_read_b128 v[50:53], v1 offset:14336\n"
    "s_waitcnt lgkmcnt(4)\n"
    "v_mfma_f32_32x32x16_bf16 v[26:41], v[184:187], v[6:9], v[26:41]\n"
    "ds_read_b128 v[6:9], v1 offset:15360\n"
    "s_cbranch_vccz .Lrt8x\n"
    ".Lrt8:\n"
    "s_cmp_eq_u32 s100, 0\n"
    "s_cbranch_scc1 .Lrt8x\n"
    "s_sub_u32 s100, s100, 1\n"
    "s_mov_b64 exec, vcc\n"
    "buffer_load_dwordx2 v[112:113], v16, s[28:31], s7 offen sc1\n"
    "s_add_u32 s20, s7, 4096\n"
    "buffer_load_dwordx2 v[114:115], v16, s[28:31], s20 offen sc1\n"
    "s_add_u32 s20, s7, 8192\n"
    "buffer_load_dwordx2 v[116:117], v16, s[28:31], s20 offen sc1\n"
    "s_add_u32 s20, s7, 12288\n"
    "s_mov_b32 exec_hi, 0\n"
    "buffer_load_dwordx2 v[118:119], v16, s[28:31], s20 offen sc1\n"
    "s_mov_b64 exec, -1\n"
    "s_mov_b64 exec, -1\n"
    "s_waitcnt vmcnt(0)\n"
    "v_max3_u32 v17, v112, v113, v114\n"
    "v_max3_u32 v17, v17, v115, v116\n"
    "v_max3_u32 v17, v17, v117, v118\n"
    "v_max_u32_e32 v17, v17, v119\n"
    "v_cmp_eq_u32_e32 vcc, -1, v17\n"
    "s_nop 4\n"
    "s_cbranch_vccnz .Lrt8\n"
    ".Lrt8x:\n"
    "s_add_u32 m0, s63, 95232\n"
    "s_add_u32 s21, s47, 7168\n"
    "buffer_load_dwordx4 v79, s[24:27], s21 offen offset:0  lds\n"
    "s_waitcnt lgkmcnt(4)\n"
    "v_mfma_f32_32x32x16_bf16 v[26:41], v[188:191], v[12:15], v[26:41]\n"
    "ds_read_b128 v[12:15], v1 offset:16384\n"
    "v_add_f32_e32 v112, v112, v114\n"
    "v_add_f32_e32 v113, v113, v115\n"
    "v_add_f32_e32 v112, v112, v116\n"
    "buffer_load_dwordx4 v[140:143], v1, s[28:31], s12 offen offset:0 sc1\n"
    "s_waitcnt lgkmcnt(4)\n"
    "v_mfma_f32_32x32x16_bf16 v[26:41], v[192:195], v[42:45], v[26:41]\n"
    "ds_read_b128 v[42:45], v1 offset:17408\n"
    "v_add_f32_e32 v113, v113, v117\n"
    "v_add_f32_e32 v112, v112, v118\n"
    "v_add_f32_e32 v113, v113, v119\n"
    "s_add_u32 m0, s63, 96256\n"
    "s_add_u32 s21, s47, 14336\n"
    "buffer_load_dwordx4 v79, s[24:27], s21 offen offset:0  lds\n"
    "s_waitcnt lgkmcnt(4)\n"
    "v_mfma_f32_32x32x16_bf16 v[26:41], v[196:199], v[46:49], v[26:41]\n"
    "ds_read_b128 v[46:49], v1 offset:18432\n"
    "v_permlane32_swap_b32_e32 v112, v113\n"
    "v_add_f32_e32 v112, v112, v113\n"
    "s_waitcnt lgkmcnt(4)\n"
    "v_mfma_f32_32x32x16_bf16 v[26:41], v[200:203], v[50:53], v[26:41]\n"
    "ds_read_b128 v[50:53], v1 offset:19456\n"
    "v_mov_b32_e32 v113, v112\n"
    "s_nop 1\n"
    "v_permlane16_swap_b32_e32 v112, v113\n"
    "v_add_f32_e32 v112, v112, v113\n"
    "s_add_u32 m0, s63, 97280\n"
    "s_add_u32 s21, s47, 21504\n"
    "buffer_load_dwordx4 v79, s[24:27], s21 offen offset:0  lds\n"
    "s_waitcnt lgkmcnt(4)\n"
    "v_mfma_f32_32x32x16_bf16 v[26:41], v[204:207], v[6:9], v[26:41]\n"
    "ds_read_b128 v[6:9], v1 offset:20480\n"
    "v_mov_b32_e32 v113, v112\n"
    "v_mov_b32_e32 v114, v112\n"
    "s_waitcnt lgkmcnt(4)\n"
    "v_mfma_f32_32x32x16_bf16 v[26:41], v[208:211], v[12:15], v[26:41]\n"
    "ds_read_b128 v[12:15], v1 offset:21504\n"
    "v_permlane32_swap_b32_e32 v114, v113\n"
    "s_lshl_b32 s52, s3, 8\n"
    "s_add_u32 s52, s52, 159744\n"
    "s_waitcnt lgkmcnt(4)\n"
    "v_mfma_f32_32x32x16_bf16 v[26:41], v[212:215], v[42:45], v[26:41]\n"
    "ds_read_b128 v[42:45], v1 offset:22528\n"
    "v_add_u32_e32 v18, s52, v1\n"
    "v_mul_f32_e32 v116, 0xbb124925, v112\n"
    "v_mul_f32_e32 v114, v116, v116\n"
    "s_waitcnt lgkmcnt(4)\n"
    "v_mfma_f32_32x32x16_bf16 v[26:41], v[216:219], v[46:49], v[26:41]\n"
    "ds_read_b128 v[46:49], v1 offset:23552\n"
    "v_mul_f32_e32 v113, 0x3b124925, v113\n"
    "v_sub_f32_e32 v113, v113, v114\n"
    "s_waitcnt lgkmcnt(4)\n"
    "v_mfma_f32_32x32x16_bf16 v[26:41], v[220:223], v[50:53], v[26:41]\n"
    "ds_read_b128 v[50:53], v1 offset:24576\n"
    "v_max_f32_e32 v113, 0, v113\n"
    "v_add_f32_e32 v118, 0x3727c5ac, v113\n"
    "v_rsq_f32_e32 v117, v118\n"
    "s_waitcnt lgkmcnt(4)\n"
    "v_mfma_f32_32x32x16_bf16 v[26:41], v[224:227], v[6:9], v[26:41]\n"
    "ds_read_b128 v[6:9], v1 offset:25600\n"
    "v_mul_f32_e32 v117, 0xbfb8aa3b, v117\n"
    "s_mov_b64 exec, 0xffff\n"
    "ds_write_b128 v18, v[116:119] offset:0\n"
    "s_mov_b64 exec, -1\n"
    "s_waitcnt lgkmcnt(5)\n"
    "v_mfma_f32_32x32x16_bf16 v[26:41], v[228:231], v[12:15], v[26:41]\n"
    "ds_read_b128 v[12:15], v1 offset:26624\n"
    "s_waitcnt lgkmcnt(5)\n"
    "v_mfma_f32_32x32x16_bf16 v[26:41], v[232:235], v[42:45], v[26:41]\n"
    "ds_read_b128 v[42:45], v1 offset:27648\n"
    "s_waitcnt lgkmcnt(2)\n"
    "s_barrier\n"
    "v_mfma_f32_32x32x16_bf16 v[26:41], v[236:239], v[46:49], v[26:41]\n"
    "ds_read_b128 v[46:49], v1 offset:28672\n"
    "ds_read_b64_tr_b16 v[112:113], v78 offset:0\n"
    "ds_read_b64_tr_b16 v[114:115], v78 offset:1024\n"
    "ds_read_b64_tr_b16 v[116:117], v77 offset:0\n"
    "ds_read_b64_tr_b16 v[118:119], v77 offset:1024\n"
    "v_mfma_f32_32x32x16_bf16 v[26:41], v[240:243], v[50:53], v[26:41]\n"
    "ds_read_b128 v[50:53], v1 offset:29696\n"
    "ds_read_b64_tr_b16 v[120:121], v76 offset:0\n"
    "ds_read_b64_tr_b16 v[122:123], v76 offset:1024\n"
    "ds_read_b64_tr_b16 v[124:125], v75 offset:0\n"
    "ds_read_b64_tr_b16 v[126:127], v75 offset:1024\n"
    "v_mfma_f32_32x32x16_bf16 v[26:41], v[244:247], v[6:9], v[26:41]\n"
    "ds_read_b128 v[6:9], v1 offset:30720\n"
    "ds_read_b128 v[54:57], v62 offset:64\n"
    "s_waitcnt lgkmcnt(13)\n"
    "v_mfma_f32_32x32x16_bf16 v[26:41], v[248:251], v[12:15], v[26:41]\n"
    "ds_read_b128 v[12:15], v1 offset:31744\n"
    "ds_read_b128 v[58:61], v62 offset:96\n"
    "s_waitcnt lgkmcnt(14)\n"
    "v_mfma_f32_32x32x16_bf16 v[26:41], v[252:255], v[42:45], v[26:41]\n"
    "ds_read_b128 v[42:45], v1 offset:32768\n"
    "ds_read_b128 v[20:23], v19 offset:0\n"
    "s_waitcnt vmcnt(2)\n"
    "v_cmp_ne_u64_e64 s[52:53], s[36:37], v[140:141]\n"
    "v_cmp_ne_u64_e64 s[54:55], s[36:37], v[142:143]\n"
    "s_nop 0\n"
    "s_or_b64 vcc, s[52:53], s[54:55]\n"
    "s_cbranch_vccz .Lrt9x\n"
    ".Lrt9:\n"
    "s_cmp_eq_u32 s100, 0\n"
    "s_cbranch_scc1 .Lrt9x\n"
    "s_sub_u32 s100, s100, 1\n"
    "s_mov_b64 exec, vcc\n"
    "buffer_load_dwordx4 v[140:143], v1, s[28:31], s12 offen offset:0 sc1\n"
    "s_mov_b64 exec, -1\n"
    "s_waitcnt vmcnt(0)\n"
    "v_cmp_ne_u64_e64 s[52:53], s[36:37], v[140:141]\n"
    "v_cmp_ne_u64_e64 s[54:55], s[36:37], v[142:143]\n"
    "s_nop 0\n"
    "s_or_b64 vcc, s[52:53], s[54:55]\n"
    "s_nop 4\n"
    "s_cbranch_vccnz .Lrt9\n"
    ".Lrt9x:\n"
    "s_add_u32 m0, s10, 126976\n"
    "s_nop 0\n"
    "buffer_load_dwordx4 v1, s[28:31], s9 offen offset:0 sc1 lds\n"
    "s_waitcnt lgkmcnt(14)\n"
    "v_mfma_f32_32x32x16_bf16 v[128:143], v[144:147], v[46:49], 0\n"
    "ds_read_b128 v[46:49], v1 offset:33792\n"
    "s_waitcnt lgkmcnt(11)\n"
    "v_mfma_f32_32x32x16_bf16 v[128:143], v[148:151], v[50:53], v[128:143]\n"
    "ds_read_b128 v[50:53], v1 offset:34816\n"
    "s_waitcnt lgkmcnt(2)\n"
    "v_fmac_f32_e32 v34, v54, v20\n"
    "v_fmac_f32_e32 v35, v55, v20\n"
    "v_fmac_f32_e32 v36, v56, v20\n"
    "s_add_u32 m0, s10, 126976\n"
    "s_nop 0\n"
    "buffer_load_dwordx4 v1, s[28:31], s9 offen offset:1024 sc1 lds\n"
    "v_mfma_f32_32x32x16_bf16 v[128:143], v[152:155], v[6:9], v[128:143]\n"
    "ds_read_b128 v[6:9], v1 offset:35840\n"
    "v_fmac_f32_e32 v37, v57, v20\n"
    "v_fmac_f32_e32 v38, v58, v20\n"
    "v_fmac_f32_e32 v39, v59, v20\n"
    "v_mfma_f32_32x32x16_bf16 v[128:143], v[156:159], v[12:15], v[128:143]\n"
    "ds_read_b128 v[12:15], v1 offset:36864\n"
    "v_fmac_f32_e32 v40, v60, v20\n"
    "v_fmac_f32_e32 v41, v61, v20\n"
    "ds_read_b128 v[54:57], v62 offset:0\n"
    "ds_read_b128 v[58:61], v62 offset:32\n"
    "s_add_u32 m0, s10, 126976\n"
    "s_nop 0\n"
    "buffer_load_dwordx4 v1, s[28:31], s9 offen offset:2048 sc1 lds\n"
    "v_mfma_f32_32x32x16_bf16 v[128:143], v[160:163], v[42:45], v[128:143]\n"
    "ds_read_b128 v[42:45], v1 offset:37888\n"
    "s_waitcnt lgkmcnt(6)\n"
    "v_mfma_f32_32x32x16_bf16 v[128:143], v[164:167], v[46:49], v[128:143]\n"
    "ds_read_b128 v[46:49], v1 offset:38912\n"
    "s_add_u32 m0, s10, 126976\n"
    "s_nop 0\n"
    "buffer_load_dwordx4 v1, s[28:31], s9 offen offset:3072 sc1 lds\n"
    "s_waitcnt lgkmcnt(6)\n"
    "v_mfma_f32_32x32x16_bf16 v[128:143], v[168:171], v[50:53], v[128:143]\n"
    "ds_read_b128 v[50:53], v1 offset:39936\n"
    "s_waitcnt lgkmcnt(4)\n"
    "v_fmac_f32_e32 v26, v54, v20\n"
    "v_fmac_f32_e32 v27, v55, v20\n"
    "v_fmac_f32_e32 v28, v56, v20\n"
    "v_fmac_f32_e32 v29, v57, v20\n"
    "v_mfma_f32_32x32x16_bf16 v[128:143], v[172:175], v[6:9], v[128:143]\n"
    "ds_read_b128 v[6:9], v1 offset:40960\n"
    "s_waitcnt lgkmcnt(4)\n"
    "v_fmac_f32_e32 v30, v58, v20\n"
    "v_fmac_f32_e32 v31, v59, v20\n"
    "v_fmac_f32_e32 v32, v60, v20\n"
    "v_fmac_f32_e32 v33, v61, v20\n"
    "v_mfma_f32_32x32x16_bf16 v[128:143], v[176:179], v[12:15], v[128:143]\n"
    "ds_read_b128 v[12:15], v1 offset:41984\n"
    "v_mul_f32_e32 v54, v26, v21\n"
    "v_mul_f32_e32 v55, v27, v21\n"
    "v_mul_f32_e32 v56, v28, v21\n"
    "v_mul_f32_e32 v57, v29, v21\n"
    "s_add_u32 m0, s63, 61440\n"
    "s_add_u32 s21, s47, 28672\n"
    "buffer_load_dwordx4 v79, s[24:27], s21 offen offset:0  lds\n"
    "s_waitcnt lgkmcnt(4)\n"
    "v_mfma_f32_32x32x16_bf16 v[128:143], v[180:183], v[42:45], v[128:143]\n"
    "ds_read_b128 v[42:45], v1 offset:43008\n"
    "v_mul_f32_e32 v58, v30, v21\n"
    "v_mul_f32_e32 v59, v31, v21\n"
    "v_mul_f32_e32 v60, v32, v21\n"
    "v_mul_f32_e32 v61, v33, v21\n"
    "s_waitcnt lgkmcnt(4)\n"
    "v_mfma_f32_32x32x16_bf16 v[128:143], v[184:187], v[46:49], v[128:143]\n"
    "ds_read_b128 v[46:49], v1 offset:44032\n"
    "v_exp_f32_e32 v54, v54\n"
    "v_mul_f32_e32 v26, v26, v34\n"
    "v_exp_f32_e32 v55, v55\n"
    "s_add_u32 m0, s63, 62464\n"
    "s_add_u32 s21, s47, 35840\n"
    "buffer_load_dwordx4 v79, s[24:27], s21 offen offset:0  lds\n"
    "s_waitcnt lgkmcnt(4)\n"
    "v_mfma_f32_32x32x16_bf16 v[128:143], v[188:191], v[50:53], v[128:143]\n"
    "ds_read_b128 v[50:53], v1 offset:45056\n"
    "v_mul_f32_e32 v27, v27, v35\n"
    "v_exp_f32_e32 v56, v56\n"
    "v_mul_f32_e32 v28, v28, v36\n"
    "v_exp_f32_e32 v57, v57\n"
    "s_waitcnt lgkmcnt(4)\n"
    "v_mfma_f32_32x32x16_bf16 v[128:143], v[192:195], v[6:9], v[128:143]\n"
    "ds_read_b128 v[6:9], v1 offset:46080\n"
    "v_mul_f32_e32 v29, v29, v37\n"
    "v_exp_f32_e32 v58, v58\n"
    "v_mul_f32_e32 v30, v30, v38\n"
    "v_exp_f32_e32 v59, v59\n"
    "s_add_u32 m0, s63, 63488\n"
    "s_add_u32 s21, s47, 43008\n"
    "buffer_load_dwordx4 v79, s[24:27], s21 offen offset:0  lds\n"
    "s_waitcnt lgkmcnt(4)\n"
    "v_mfma_f32_32x32x16_bf16 v[128:143], v[196:199], v[12:15], v[128:143]\n"
    "ds_read_b128 v[12:15], v1 offset:47104\n"
    "v_mul_f32_e32 v31, v31, v39\n"
    "v_exp_f32_e32 v60, v60\n"
    "v_mul_f32_e32 v32, v32, v40\n"
    "v_exp_f32_e32 v61, v61\n"
    "s_waitcnt lgkmcnt(4)\n"
    "v_mfma_f32_32x32x16_bf16 v[128:143], v[200:203], v[42:45], v[128:143]\n"
    "ds_read_b128 v[42:45], v1 offset:48128\n"
    "v_mul_f32_e32 v33, v33, v41\n"
    "v_fma_f32 v54, v54, v22, v22\n"
    "v_fma_f32 v55, v55, v22, v22\n"
    "s_add_u32 m0, s63, 64512\n"
    "s_add_u32 s21, s47, 50176\n"
    "buffer_load_dwordx4 v79, s[24:27], s21 offen offset:0  lds\n"
    "s_waitcnt lgkmcnt(4)\n"
    "v_mfma_f32_32x32x16_bf16 v[128:143], v[204:207], v[46:49], v[128:143]\n"
    "ds_read_b128 v[46:49], v1 offset:49152\n"
    "v_fma_f32 v56, v56, v22, v22\n"
    "v_rcp_f32_e32 v54, v54\n"
    "v_fma_f32 v57, v57, v22, v22\n"
    "v_rcp_f32_e32 v55, v55\n"
    "s_waitcnt lgkmcnt(4)\n"
    "v_mfma_f32_32x32x16_bf16 v[128:143], v[208:211], v[50:53], v[128:143]\n"
    "ds_read_b128 v[50:53], v1 offset:50176\n"
    "v_fma_f32 v58, v58, v22, v22\n"
    "v_rcp_f32_e32 v56, v56\n"
    "v_fma_f32 v59, v59, v22, v22\n"
    "v_rcp_f32_e32 v57, v57\n"
    "s_waitcnt lgkmcnt(4)\n"
    "v_mfma_f32_32x32x16_bf16 v[128:143], v[212:215], v[6:9], v[128:143]\n"
    "ds_read_b128 v[6:9], v1 offset:51200\n"
    "v_fma_f32 v60, v60, v22, v22\n"
    "v_rcp_f32_e32 v58, v58\n"
    "v_fma_f32 v61, v61, v22, v22\n"
    "v_rcp_f32_e32 v59, v59\n"
    "s_waitcnt lgkmcnt(4)\n"
    "v_mfma_f32_32x32x16_bf16 v[128:143], v[216:219], v[12:15], v[128:143]\n"
    "ds_read_b128 v[12:15], v1 offset:52224\n"
    "v_rcp_f32_e32 v60, v60\n"
    "v_mul_f32_e32 v26, v26, v54\n"
    "v_rcp_f32_e32 v61, v61\n"
    "s_waitcnt lgkmcnt(4)\n"
    "v_mfma_f32_32x32x16_bf16 v[128:143], v[220:223], v[42:45], v[128:143]\n"
    "ds_read_b128 v[42:45], v1 offset:53248\n"
    "v_mul_f32_e32 v27, v27, v55\n"
    "v_mul_f32_e32 v28, v28, v56\n"
    "v_mul_f32_e32 v29, v29, v57\n"
    "v_mul_f32_e32 v30, v30, v58\n"
    "s_waitcnt lgkmcnt(4)\n"
    "v_mfma_f32_32x32x16_bf16 v[128:143], v[224:227], v[46:49], v[128:143]\n"
    "ds_read_b128 v[46:49], v1 offset:54272\n"
    "v_mul_f32_e32 v31, v31, v59\n"
    "v_mul_f32_e32 v32, v32, v60\n"
    "v_mul_f32_e32 v33, v33, v61\n"
    "v_cvt_pk_bf16_f32 v54, v26, v27\n"
    "s_waitcnt lgkmcnt(4)\n"
    "v_mfma_f32_32x32x16_bf16 v[128:143], v[228:231], v[50:53], v[128:143]\n"
    "ds_read_b128 v[50:53], v1 offset:55296\n"
    "v_cvt_pk_bf16_f32 v55, v28, v29\n"
    "v_cvt_pk_bf16_f32 v56, v30, v31\n"
    "v_cvt_pk_bf16_f32 v57, v32, v33\n"
    "v_min_u32_e32 v54, -2, v54\n"
    "s_waitcnt lgkmcnt(4)\n"
    "v_mfma_f32_32x32x16_bf16 v[128:143], v[232:235], v[6:9], v[128:143]\n"
    "ds_read_b128 v[6:9], v1 offset:56320\n"
    "v_min_u32_e32 v55, -2, v55\n"
    "v_min_u32_e32 v56, -2, v56\n"
    "v_min_u32_e32 v57, -2, v57\n"
    "s_waitcnt vmcnt(4)\n"
    "s_barrier\n"
    "s_add_u32 s9, s9, 28672\n"
    "s_waitcnt lgkmcnt(4)\n"
    "v_mfma_f32_32x32x16_bf16 v[128:143], v[236:239], v[12:15], v[128:143]\n"
    "ds_read_b128 v[12:15], v10 offset:0\n"
    "buffer_store_dwordx4 v[54:57], v1, s[28:31], s11 offen sc1\n"
    "s_add_u32 s11, s11, 114688\n"
    "s_waitcnt lgkmcnt(4)\n"
    "v_mfma_f32_32x32x16_bf16 v[128:143], v[240:243], v[42:45], v[128:143]\n"
    "ds_read_b128 v[42:45], v10 offset:1024\n"
    "s_add_u32 m0, s10, 0\n"
    "s_nop 0\n"
    "buffer_load_dwordx4 v1, s[28:31], s9 offen offset:0 sc1 lds\n"
    "s_waitcnt lgkmcnt(4)\n"
    "v_mfma_f32_32x32x16_bf16 v[128:143], v[244:247], v[46:49], v[128:143]\n"
    "ds_read_b128 v[46:49], v10 offset:2048\n"
    "ds_read_b128 v[54:57], v62 offset:64\n"
    "s_waitcnt lgkmcnt(5)\n"
    "v_mfma_f32_32x32x16_bf16 v[128:143], v[248:251], v[50:53], v[128:143]\n"
    "ds_read_b128 v[50:53], v10 offset:3072\n"
    "ds_read_b128 v[58:61], v62 offset:96\n"
    "s_add_u32 m0, s10, 0\n"
    "s_nop 0\n"
    "buffer_load_dwordx4 v1, s[28:31], s9 offen offset:1024 sc1 lds\n"
    "s_waitcnt lgkmcnt(6)\n"
    "v_mfma_f32_32x32x16_bf16 v[128:143], v[252:255], v[6:9], v[128:143]\n"
    "ds_read_b128 v[6:9], v10 offset:4096\n"
    "ds_read_b128 v[20:23], v19 offset:512\n"
    "s_waitcnt lgkmcnt(7)\n"
    "v_mfma_f32_32x32x16_bf16 v[26:41], v[144:147], v[12:15], 0\n"
    "ds_read_b128 v[12:15], v10 offset:5120\n"
    "s_add_u32 m0, s10, 0\n"
    "s_nop 0\n"
    "buffer_load_dwordx4 v1, s[28:31], s9 offen offset:2048 sc1 lds\n"
    "s_waitcnt lgkmcnt(7)\n"
    "v_mfma_f32_32x32x16_bf16 v[26:41], v[148:151], v[42:45], v[26:41]\n"
    "ds_read_b128 v[42:45], v10 offset:6144\n"
    "s_waitcnt lgkmcnt(2)\n"
    "v_fmac_f32_e32 v136, v54, v20\n"
    "v_fmac_f32_e32 v137, v55, v20\n"
    "v_fmac_f32_e32 v138, v56, v20\n"
    "v_mfma_f32_32x32x16_bf16 v[26:41], v[152:155], v[46:49], v[26:41]\n"
    "ds_read_b128 v[46:49], v10 offset:7168\n"
    "v_fmac_f32_e32 v139, v57, v20\n"
    "v_fmac_f32_e32 v140, v58, v20\n"
    "v_fmac_f32_e32 v141, v59, v20\n"
    "s_add_u32 m0, s10, 0\n"
    "s_nop 0\n"
    "buffer_load_dwordx4 v1, s[28:31], s9 offen offset:3072 sc1 lds\n"
    "v_mfma_f32_32x32x16_bf16 v[26:41], v[156:159], v[50:53], v[26:41]\n"
    "ds_read_b128 v[50:53], v10 offset:8192\n"
    "v_fmac_f32_e32 v142, v60, v20\n"
    "v_fmac_f32_e32 v143, v61, v20\n"
    "ds_read_b128 v[54:57], v62 offset:0\n"
    "ds_read_b128 v[58:61], v62 offset:32\n"
    "v_mfma_f32_32x32x16_bf16 v[26:41], v[160:163], v[6:9], v[26:41]\n"
    "ds_read_b128 v[6:9], v10 offset:9216\n"
    "s_barrier\n"
    "s_waitcnt lgkmcnt(6)\n"
    "v_mfma_f32_32x32x16_bf16 v[26:41], v[164:167], v[12:15], v[26:41]\n"
    "ds_read_b128 v[12:15], v10 offset:10240\n"
    "s_waitcnt lgkmcnt(6)\n"
    "v_mfma_f32_32x32x16_bf16 v[26:41], v[168:171], v[42:45], v[26:41]\n"
    "ds_read_b128 v[42:45], v10 offset:11264\n"
    "s_waitcnt lgkmcnt(4)\n"
    "v_fmac_f32_e32 v128, v54, v20\n"
    "v_fmac_f32_e32 v129, v55, v20\n"
    "v_fmac_f32_e32 v130, v56, v20\n"
    "v_fmac_f32_e32 v131, v57, v20\n"
    "s_add_u32 m0, s63, 28672\n"
    "s_add_u32 s21, s47, 57344\n"
    "buffer_load_dwordx4 v79, s[24:27], s21 offen offset:0  lds\n"
    "v_mfma_f32_32x32x16_bf16 v[26:41], v[172:175], v[46:49], v[26:41]\n"
    "ds_read_b128 v[46:49], v10 offset:12288\n"
    "s_waitcnt lgkmcnt(4)\n"
    "v_fmac_f32_e32 v132, v58, v20\n"
    "v_fmac_f32_e32 v133, v59, v20\n"
    "v_fmac_f32_e32 v134, v60, v20\n"
    "v_fmac_f32_e32 v135, v61, v20\n"
    "v_mfma_f32_32x32x16_bf16 v[26:41], v[176:179], v[50:53], v[26:41]\n"
    "ds_read_b128 v[50:53], v10 offset:13312\n"
    "v_mul_f32_e32 v54, v128, v21\n"
    "v_mul_f32_e32 v55, v129, v21\n"
    "v_mul_f32_e32 v56, v130, v21\n"
    "v_mul_f32_e32 v57, v131, v21\n"
    "s_add_u32 m0, s63, 29696\n"
    "s_add_u32 s21, s47, 64512\n"
    "buffer_load_dwordx4 v79, s[24:27], s21 offen offset:0  lds\n"
    "s_waitcnt lgkmcnt(4)\n"
    "v_mfma_f32_32x32x16_bf16 v[26:41], v[180:183], v[6:9], v[26:41]\n"
    "ds_read_b128 v[6:9], v10 offset:14336\n"
    "v_mul_f32_e32 v58, v132, v21\n"
    "v_mul_f32_e32 v59, v133, v21\n"
    "v_mul_f32_e32 v60, v134, v21\n"
    "v_mul_f32_e32 v61, v135, v21\n"
    "s_waitcnt lgkmcnt(4)\n"
    "v_mfma_f32_32x32x16_bf16 v[26:41], v[184:187], v[12:15], v[26:41]\n"
    "ds_read_b128 v[12:15], v10 offset:15360\n"
    "v_exp_f32_e32 v54, v54\n"
    "v_mul_f32_e32 v128, v128, v136\n"
    "v_exp_f32_e32 v55, v55\n"
    "s_waitcnt lgkmcnt(4)\n"
    "v_mfma_f32_32x32x16_bf16 v[26:41], v[188:191], v[42:45], v[26:41]\n"
    "ds_read_b128 v[42:45], v10 offset:16384\n"
    "v_mul_f32_e32 v129, v129, v137\n"
    "v_exp_f32_e32 v56, v56\n"
    "v_mul_f32_e32 v130, v130, v138\n"
    "v_exp_f32_e32 v57, v57\n"
    "s_add_u32 m0, s63, 30720\n"
    "s_add_u32 s21, s47, 71680\n"
    "buffer_load_dwordx4 v79, s[24:27], s21 offen offset:0  lds\n"
    "s_waitcnt lgkmcnt(4)\n"
    "v_mfma_f32_32x32x16_bf16 v[26:41], v[192:195], v[46:49], v[26:41]\n"
    "ds_read_b128 v[46:49], v10 offset:17408\n"
    "v_mul_f32_e32 v131, v131, v139\n"
    "v_exp_f32_e32 v58, v58\n"
    "v_mul_f32_e32 v132, v132, v140\n"
    "v_exp_f32_e32 v59, v59\n"
    "s_waitcnt lgkmcnt(4)\n"
    "v_mfma_f32_32x32x16_bf16 v[26:41], v[196:199], v[50:53], v[26:41]\n"
    "ds_read_b128 v[50:53], v10 offset:18432\n"
    "v_mul_f32_e32 v133, v133, v141\n"
    "v_exp_f32_e32 v60, v60\n"
    "v_mul_f32_e32 v134, v134, v142\n"
    "v_exp_f32_e32 v61, v61\n"
    "s_waitcnt lgkmcnt(4)\n"
    "v_mfma_f32_32x32x16_bf16 v[26:41], v[200:203], v[6:9], v[26:41]\n"
    "ds_read_b128 v[6:9], v10 offset:19456\n"
    "v_mul_f32_e32 v135, v135, v143\n"
    "v_fma_f32 v54, v54, v22, v22\n"
    "v_fma_f32 v55, v55, v22, v22\n"
    "s_add_u32 m0, s63, 31744\n"
    "s_add_u32 s21, s47, 78848\n"
    "buffer_load_dwordx4 v79, s[24:27], s21 offen offset:0  lds\n"
    "s_waitcnt lgkmcnt(4)\n"
    "v_mfma_f32_32x32x16_bf16 v[26:41], v[204:207], v[12:15], v[26:41]\n"
    "ds_read_b128 v[12:15], v10 offset:20480\n"
    "v_fma_f32 v56, v56, v22, v22\n"
    "v_rcp_f32_e32 v54, v54\n"
    "v_fma_f32 v57, v57, v22, v22\n"
    "v_rcp_f32_e32 v55, v55\n"
    "s_waitcnt lgkmcnt(4)\n"
    "v_mfma_f32_32x32x16_bf16 v[26:41], v[208:211], v[42:45], v[26:41]\n"
    "ds_read_b128 v[42:45], v10 offset:21504\n"
    "v_fma_f32 v58, v58, v22, v22\n"
    "v_rcp_f32_e32 v56, v56\n"
    "v_fma_f32 v59, v59, v22, v22\n"
    "v_rcp_f32_e32 v57, v57\n"
    "s_waitcnt lgkmcnt(4)\n"
    "v_mfma_f32_32x32x16_bf16 v[26:41], v[212:215], v[46:49], v[26:41]\n"
    "ds_read_b128 v[46:49], v10 offset:22528\n"
    "v_fma_f32 v60, v60, v22, v22\n"
    "v_rcp_f32_e32 v58, v58\n"
    "v_fma_f32 v61, v61, v22, v22\n"
    "v_rcp_f32_e32 v59, v59\n"
    "s_waitcnt lgkmcnt(4)\n"
    "v_mfma_f32_32x32x16_bf16 v[26:41], v[216:219], v[50:53], v[26:41]\n"
    "ds_read_b128 v[50:53], v10 offset:23552\n"
    "v_rcp_f32_e32 v60, v60\n"
    "v_mul_f32_e32 v128, v128, v54\n"
    "v_rcp_f32_e32 v61, v61\n"
    "s_waitcnt lgkmcnt(4)\n"
    "v_mfma_f32_32x32x16_bf16 v[26:41], v[220:223], v[6:9], v[26:41]\n"
    "ds_read_b128 v[6:9], v10 offset:24576\n"
    "v_mul_f32_e32 v129, v129, v55\n"
    "v_mul_f32_e32 v130, v130, v56\n"
    "v_mul_f32_e32 v131, v131, v57\n"
    "v_mul_f32_e32 v132, v132, v58\n"
    "s_waitcnt lgkmcnt(4)\n"
    "v_mfma_f32_32x32x16_bf16 v[26:41], v[224:227], v[12:15], v[26:41]\n"
    "ds_read_b128 v[12:15], v10 offset:25600\n"
    "v_mul_f32_e32 v133, v133, v59\n"
    "v_mul_f32_e32 v134, v134, v60\n"
    "v_mul_f32_e32 v135, v135, v61\n"
    "v_cvt_pk_bf16_f32 v54, v128, v129\n"
    "s_waitcnt lgkmcnt(4)\n"
    "v_mfma_f32_32x32x16_bf16 v[26:41], v[228:231], v[42:45], v[26:41]\n"
    "ds_read_b128 v[42:45], v10 offset:26624\n"
    "v_cvt_pk_bf16_f32 v55, v130, v131\n"
    "v_cvt_pk_bf16_f32 v56, v132, v133\n"
    "v_cvt_pk_bf16_f32 v57, v134, v135\n"
    "v_min_u32_e32 v54, -2, v54\n"
    "s_waitcnt lgkmcnt(4)\n"
    "v_mfma_f32_32x32x16_bf16 v[26:41], v[232:235], v[46:49], v[26:41]\n"
    "ds_read_b128 v[46:49], v10 offset:27648\n"
    "v_min_u32_e32 v55, -2, v55\n"
    "v_min_u32_e32 v56, -2, v56\n"
    "v_min_u32_e32 v57, -2, v57\n"
    "s_waitcnt vmcnt(4)\n"
    "s_barrier\n"
    "s_waitcnt lgkmcnt(4)\n"
    "v_mfma_f32_32x32x16_bf16 v[26:41], v[236:239], v[50:53], v[26:41]\n"
    "ds_read_b128 v[50:53], v1 offset:0\n"
    "buffer_store_dwordx4 v[54:57], v1, s[28:31], s11 offen sc1\n"
    "s_add_u32 s11, s11, 114688\n"
    "s_waitcnt lgkmcnt(4)\n"
    "v_mfma_f32_32x32x16_bf16 v[26:41], v[240:243], v[6:9], v[26:41]\n"
    "ds_read_b128 v[6:9], v1 offset:1024\n"
    "v_lshrrev_b32_e32 v17, 5, v0\n"
    "v_and_b32_e32 v11, 1, v3\n"
    "s_waitcnt lgkmcnt(4)\n"
    "v_mfma_f32_32x32x16_bf16 v[26:41], v[244:247], v[12:15], v[26:41]\n"
    "ds_read_b128 v[12:15], v1 offset:2048\n"
    "ds_read_b128 v[54:57], v62 offset:64\n"
    "v_lshlrev_b32_e32 v18, 10, v17\n"
    "s_waitcnt lgkmcnt(5)\n"
    "v_mfma_f32_32x32x16_bf16 v[26:41], v[248:251], v[42:45], v[26:41]\n"
    "ds_read_b128 v[42:45], v1 offset:3072\n"
    "ds_read_b128 v[58:61], v62 offset:96\n"
    "v_lshl_add_u32 v18, v11, 9, v18\n"
    "s_waitcnt lgkmcnt(6)\n"
    "v_mfma_f32_32x32x16_bf16 v[26:41], v[252:255], v[46:49], v[26:41]\n"
    "ds_read_b128 v[46:49], v1 offset:4096\n"
    "ds_read_b128 v[20:23], v19 offset:1024\n"
    "v_lshl_add_u32 v18, v2, 4, v18\n"
    "s_waitcnt lgkmcnt(7)\n"
    "v_mfma_f32_32x32x16_bf16 v[128:143], v[144:147], v[50:53], 0\n"
    "ds_read_b128 v[50:53], v1 offset:5120\n"
    "s_waitcnt lgkmcnt(7)\n"
    "v_mfma_f32_32x32x16_bf16 v[128:143], v[148:151], v[6:9], v[128:143]\n"
    "ds_read_b128 v[6:9], v1 offset:6144\n"
    "s_waitcnt lgkmcnt(2)\n"
    "s_nop 1\n"
    "v_fmac_f32_e32 v34, v54, v20\n"
    "v_fmac_f32_e32 v35, v55, v20\n"
    "v_fmac_f32_e32 v36, v56, v20\n"
    "s_add_u32 s23, s16, 0\n"
    "buffer_load_dwordx4 v[144:147], v18, s[28:31], s23 offen offset:0 sc1\n"
    "v_mfma_f32_32x32x16_bf16 v[128:143], v[152:155], v[12:15], v[128:143]\n"
    "ds_read_b128 v[12:15], v1 offset:7168\n"
    "v_fmac_f32_e32 v37, v57, v20\n"
    "v_fmac_f32_e32 v38, v58, v20\n"
    "v_fmac_f32_e32 v39, v59, v20\n"
    "v_mfma_f32_32x32x16_bf16 v[128:143], v[156:159], v[42:45], v[128:143]\n"
    "ds_read_b128 v[42:45], v1 offset:8192\n"
    "v_fmac_f32_e32 v40, v60, v20\n"
    "v_fmac_f32_e32 v41, v61, v20\n"
    "ds_read_b128 v[54:57], v62 offset:0\n"
    "ds_read_b128 v[58:61], v62 offset:32\n"
    "s_add_u32 s23, s16, 2048\n"
    "buffer_load_dwordx4 v[148:151], v18, s[28:31], s23 offen offset:0 sc1\n"
    "v_mfma_f32_32x32x16_bf16 v[128:143], v[160:163], v[46:49], v[128:143]\n"
    "ds_read_b128 v[46:49], v1 offset:9216\n"
    "s_lshl_b32 s52, s56, 2\n"
    "v_add_u32_e32 v11, s52, v3\n"
    "v_xor_b32_e32 v11, v11, v2\n"
    "v_lshlrev_b32_e32 v11, 4, v11\n"
    "v_lshl_add_u32 v11, v2, 8, v11\n"
    "s_waitcnt lgkmcnt(6)\n"
    "v_mfma_f32_32x32x16_bf16 v[128:143], v[164:167], v[50:53], v[128:143]\n"
    "ds_read_b128 v[50:53], v1 offset:10240\n"
    "s_add_u32 s23, s16, 4096\n"
    "buffer_load_dwordx4 v[152:155], v18, s[28:31], s23 offen offset:0 sc1\n"
    "s_lshl_b32 s52, s57, 15\n"
    "s_add_u32 s52, s52, 28672\n"
    "v_add_u32_e32 v10, s52, v11\n"
    "s_waitcnt lgkmcnt(6)\n"
    "v_mfma_f32_32x32x16_bf16 v[128:143], v[168:171], v[6:9], v[128:143]\n"
    "ds_read_b128 v[6:9], v1 offset:11264\n"
    "s_waitcnt lgkmcnt(4)\n"
    "v_fmac_f32_e32 v26, v54, v20\n"
    "v_fmac_f32_e32 v27, v55, v20\n"
    "v_fmac_f32_e32 v28, v56, v20\n"
    "v_fmac_f32_e32 v29, v57, v20\n"
    "v_mfma_f32_32x32x16_bf16 v[128:143], v[172:175], v[12:15], v[128:143]\n"
    "ds_read_b128 v[12:15], v1 offset:12288\n"
    "s_waitcnt lgkmcnt(4)\n"
    "v_fmac_f32_e32 v30, v58, v20\n"
    "v_fmac_f32_e32 v31, v59, v20\n"
    "v_fmac_f32_e32 v32, v60, v20\n"
    "v_fmac_f32_e32 v33, v61, v20\n"
    "s_add_u32 s23, s16, 6144\n"
    "buffer_load_dwordx4 v[156:159], v18, s[28:31], s23 offen offset:0 sc1\n"
    "v_mfma_f32_32x32x16_bf16 v[128:143], v[176:179], v[42:45], v[128:143]\n"
    "ds_read_b128 v[42:45], v1 offset:13312\n"
    "v_mul_f32_e32 v54, v26, v21\n"
    "v_mul_f32_e32 v55, v27, v21\n"
    "v_mul_f32_e32 v56, v28, v21\n"
    "v_mul_f32_e32 v57, v29, v21\n"
    "s_waitcnt lgkmcnt(4)\n"
    "v_mfma_f32_32x32x16_bf16 v[128:143], v[180:183], v[46:49], v[128:143]\n"
    "ds_read_b128 v[46:49], v1 offset:14336\n"
    "v_mul_f32_e32 v58, v30, v21\n"
    "v_mul_f32_e32 v59, v31, v21\n"
    "v_mul_f32_e32 v60, v32, v21\n"
    "v_mul_f32_e32 v61, v33, v21\n"
    "s_add_u32 s23, s16, 8192\n"
    "buffer_load_dwordx4 v[160:163], v18, s[28:31], s23 offen offset:0 sc1\n"
    "s_waitcnt lgkmcnt(4)\n"
    "v_mfma_f32_32x32x16_bf16 v[128:143], v[184:187], v[50:53], v[128:143]\n"
    "ds_read_b128 v[50:53], v1 offset:15360\n"
    "v_exp_f32_e32 v54, v54\n"
    "v_mul_f32_e32 v26, v26, v34\n"
    "v_exp_f32_e32 v55, v55\n"
    "s_waitcnt lgkmcnt(4)\n"
    "v_mfma_f32_32x32x16_bf16 v[128:143], v[188:191], v[6:9], v[128:143]\n"
    "ds_read_b128 v[6:9], v1 offset:16384\n"
    "v_mul_f32_e32 v27, v27, v35\n"
    "v_exp_f32_e32 v56, v56\n"
    "v_mul_f32_e32 v28, v28, v36\n"
    "v_exp_f32_e32 v57, v57\n"
    "s_add_u32 s23, s16, 10240\n"
    "buffer_load_dwordx4 v[164:167], v18, s[28:31], s23 offen offset:0 sc1\n"
    "ds_read_b64_tr_b16 v[172:173], v78 offset:0\n"
    "ds_read_b64_tr_b16 v[174:175], v78 offset:1024\n"
    "ds_read_b64_tr_b16 v[176:177], v77 offset:0\n"
    "ds_read_b64_tr_b16 v[178:179], v77 offset:1024\n"
    "s_waitcnt lgkmcnt(8)\n"
    "v_mfma_f32_32x32x16_bf16 v[128:143], v[192:195], v[12:15], v[128:143]\n"
    "ds_read_b128 v[12:15], v1 offset:17408\n"
    "v_mul_f32_e32 v29, v29, v37\n"
    "v_exp_f32_e32 v58, v58\n"
    "v_mul_f32_e32 v30, v30, v38\n"
    "v_exp_f32_e32 v59, v59\n"
    "ds_read_b64_tr_b16 v[180:181], v76 offset:0\n"
    "ds_read_b64_tr_b16 v[182:183], v76 offset:1024\n"
    "ds_read_b64_tr_b16 v[184:185], v75 offset:0\n"
    "ds_read_b64_tr_b16 v[186:187], v75 offset:1024\n"
    "s_waitcnt lgkmcnt(12)\n"
    "v_mfma_f32_32x32x16_bf16 v[128:143], v[196:199], v[42:45], v[128:143]\n"
    "ds_read_b128 v[42:45], v1 offset:18432\n"
    "v_mul_f32_e32 v31, v31, v39\n"
    "v_exp_f32_e32 v60, v60\n"
    "v_mul_f32_e32 v32, v32, v40\n"
    "v_exp_f32_e32 v61, v61\n"
    "s_add_u32 s23, s16, 12288\n"
    "buffer_load_dwordx4 v[168:171], v18, s[28:31], s23 offen offset:0 sc1\n"
    "s_waitcnt lgkmcnt(12)\n"
    "v_mfma_f32_32x32x16_bf16 v[128:143], v[200:203], v[46:49], v[128:143]\n"
    "ds_read_b128 v[46:49], v1 offset:19456\n"
    "v_mul_f32_e32 v33, v33, v41\n"
    "v_fma_f32 v54, v54, v22, v22\n"
    "v_fma_f32 v55, v55, v22, v22\n"
    "s_waitcnt lgkmcnt(12)\n"
    "v_mfma_f32_32x32x16_bf16 v[128:143], v[204:207], v[50:53], v[128:143]\n"
    "ds_read_b128 v[50:53], v1 offset:20480\n"
    "v_fma_f32 v56, v56, v22, v22\n"
    "v_rcp_f32_e32 v54, v54\n"
    "v_fma_f32 v57, v57, v22, v22\n"
    "v_rcp_f32_e32 v55, v55\n"
    "s_waitcnt lgkmcnt(3)\n"
    "s_add_u32 m0, s63, 61440\n"
    "s_add_u32 s21, s47, 86016\n"
    "buffer_load_dwordx4 v79, s[24:27], s21 offen offset:0  lds\n"
    "ds_read_b64_tr_b16 v[188:189], v78 offset:32768\n"
    "ds_read_b64_tr_b16 v[190:191], v78 offset:33792\n"
    "ds_read_b64_tr_b16 v[192:193], v77 offset:32768\n"
    "ds_read_b64_tr_b16 v[194:195], v77 offset:33792\n"
    "v_mfma_f32_32x32x16_bf16 v[128:143], v[208:211], v[6:9], v[128:143]\n"
    "ds_read_b128 v[6:9], v1 offset:21504\n"
    "v_fma_f32 v58, v58, v22, v22\n"
    "v_rcp_f32_e32 v56, v56\n"
    "v_fma_f32 v59, v59, v22, v22\n"
    "v_rcp_f32_e32 v57, v57\n"
    "ds_read_b64_tr_b16 v[196:197], v76 offset:32768\n"
    "ds_read_b64_tr_b16 v[198:199], v76 offset:33792\n"
    "ds_read_b64_tr_b16 v[200:201], v75 offset:32768\n"
    "ds_read_b64_tr_b16 v[202:203], v75 offset:33792\n"
    "v_mfma_f32_32x32x16_bf16 v[128:143], v[212:215], v[12:15], v[128:143]\n"
    "ds_read_b128 v[12:15], v1 offset:22528\n"
    "v_fma_f32 v60, v60, v22, v22\n"
    "v_rcp_f32_e32 v58, v58\n"
    "v_fma_f32 v61, v61, v22, v22\n"
    "v_rcp_f32_e32 v59, v59\n"
    "s_add_u32 m0, s63, 62464\n"
    "s_add_u32 s21, s47, 93184\n"
    "buffer_load_dwordx4 v79, s[24:27], s21 offen offset:0  lds\n"
    "s_waitcnt lgkmcnt(12)\n"
    "v_mfma_f32_32x32x16_bf16 v[128:143], v[216:219], v[42:45], v[128:143]\n"
    "ds_read_b128 v[42:45], v1 offset:23552\n"
    "v_rcp_f32_e32 v60, v60\n"
    "v_mul_f32_e32 v26, v26, v54\n"
    "v_rcp_f32_e32 v61, v61\n"
    "v_subrev_u32_e32 v78, 32768, v78\n"
    "v_subrev_u32_e32 v77, 32768, v77\n"
    "v_subrev_u32_e32 v76, 32768, v76\n"
    "v_subrev_u32_e32 v75, 32768, v75\n"
    "s_waitcnt lgkmcnt(12)\n"
    "v_mfma_f32_32x32x16_bf16 v[128:143], v[220:223], v[46:49], v[128:143]\n"
    "ds_read_b128 v[46:49], v1 offset:24576\n"
    "v_mul_f32_e32 v27, v27, v55\n"
    "v_mul_f32_e32 v28, v28, v56\n"
    "v_mul_f32_e32 v29, v29, v57\n"
    "v_mul_f32_e32 v30, v30, v58\n"
    "s_add_u32 m0, s63, 63488\n"
    "s_add_u32 s21, s47, 100352\n"
    "buffer_load_dwordx4 v79, s[24:27], s21 offen offset:0  lds\n"
    "s_waitcnt vmcnt(11)\n"
    "ds_read_b64_tr_b16 v[204:205], v78 offset:0\n"
    "ds_read_b64_tr_b16 v[206:207], v78 offset:1024\n"
    "ds_read_b64_tr_b16 v[208:209], v77 offset:0\n"
    "ds_read_b64_tr_b16 v[210:211], v77 offset:1024\n"
    "s_waitcnt lgkmcnt(14)\n"
    "v_mfma_f32_32x32x16_bf16 v[128:143], v[224:227], v[50:53], v[128:143]\n"
    "ds_read_b128 v[50:53], v1 offset:25600\n"
    "v_mul_f32_e32 v31, v31, v59\n"
    "v_mul_f32_e32 v32, v32, v60\n"
    "v_mul_f32_e32 v33, v33, v61\n"
    "v_cvt_pk_bf16_f32 v54, v26, v27\n"
    "ds_read_b64_tr_b16 v[212:213], v76 offset:0\n"
    "ds_read_b64_tr_b16 v[214:215], v76 offset:1024\n"
    "ds_read_b64_tr_b16 v[216:217], v75 offset:0\n"
    "ds_read_b64_tr_b16 v[218:219], v75 offset:1024\n"
    "s_waitcnt lgkmcnt(14)\n"
    "v_mfma_f32_32x32x16_bf16 v[128:143], v[228:231], v[6:9], v[128:143]\n"
    "ds_read_b128 v[6:9], v1 offset:26624\n"
    "v_cvt_pk_bf16_f32 v55, v28, v29\n"
    "v_cvt_pk_bf16_f32 v56, v30, v31\n"
    "v_cvt_pk_bf16_f32 v57, v32, v33\n"
    "v_min_u32_e32 v54, -2, v54\n"
    "s_add_u32 m0, s63, 64512\n"
    "s_add_u32 s21, s47, 107520\n"
    "buffer_load_dwordx4 v79, s[24:27], s21 offen offset:0  lds\n"
    "s_waitcnt lgkmcnt(12)\n"
    "v_mfma_f32_32x32x16_bf16 v[128:143], v[232:235], v[12:15], v[128:143]\n"
    "ds_read_b128 v[12:15], v1 offset:27648\n"
    "v_min_u32_e32 v55, -2, v55\n"
    "v_min_u32_e32 v56, -2, v56\n"
    "v_min_u32_e32 v57, -2, v57\n"
    "s_barrier\n"
    "s_setprio 1\n"
    "s_waitcnt lgkmcnt(12)\n"
    "v_mfma_f32_32x32x16_bf16 v[128:143], v[236:239], v[42:45], v[128:143]\n"
    "buffer_store_dwordx4 v[54:57], v1, s[28:31], s11 offen sc1\n"
    "s_add_u32 s11, s11, 114688\n"
    "ds_read_b128 v[42:45], v62 offset:0\n"
    "s_waitcnt lgkmcnt(12)\n"
    "v_mfma_f32_32x32x16_bf16 v[128:143], v[240:243], v[46:49], v[128:143]\n"
    "ds_read_b128 v[46:49], v62 offset:32\n"
    "s_add_u32 s23, s16, 114688\n"
    "buffer_load_dwordx4 v[220:223], v18, s[28:31], s23 offen offset:0 sc1\n"
    "s_waitcnt lgkmcnt(8)\n"
    "v_mfma_f32_32x32x16_bf16 v[128:143], v[244:247], v[50:53], v[128:143]\n"
    "ds_read_b128 v[54:57], v62 offset:64\n"
    "s_cmp_eq_u32 s56, 0\n"
    "s_mov_b32 s52, 0x0c0c0c0c\n"
    "s_cselect_b32 s18, 0x01000c0c, s52\n"
    "s_cselect_b32 s19, 0x03020c0c, s52\n"
    "v_perm_b32 v26, v70, v70, s18\n"
    "v_perm_b32 v27, v70, v70, s19\n"
    "s_waitcnt lgkmcnt(4)\n"
    "v_mfma_f32_32x32x16_bf16 v[128:143], v[248:251], v[6:9], v[128:143]\n"
    "ds_read_b128 v[58:61], v62 offset:96\n"
    "s_add_u32 s23, s16, 116736\n"
    "buffer_load_dwordx4 v[224:227], v18, s[28:31], s23 offen offset:0 sc1\n"
    "v_perm_b32 v28, v71, v71, s18\n"
    "v_perm_b32 v29, v71, v71, s19\n"
    "v_perm_b32 v30, v72, v72, s18\n"
    "s_waitcnt lgkmcnt(4)\n"
    "v_mfma_f32_32x32x16_bf16 v[128:143], v[252:255], v[12:15], v[128:143]\n"
    "ds_read_b128 v[20:23], v19 offset:1536\n"
    "v_perm_b32 v31, v72, v72, s19\n"
    "v_perm_b32 v32, v73, v73, s18\n"
    "v_perm_b32 v33, v73, v73, s19\n"
    "s_waitcnt vmcnt(13)\n"
    "v_mfma_f32_16x16x32_bf16 v[34:37], v[88:91], v[144:147], 0\n"
    "v_max3_u32 v17, v144, v145, v146\n"
    "s_waitcnt lgkmcnt(0)\n"
    "s_nop 3\n"
    "v_fmac_f32_e32 v136, v54, v20\n"
    "v_fmac_f32_e32 v137, v55, v20\n"
    "v_fmac_f32_e32 v138, v56, v20\n"
    "v_fmac_f32_e32 v139, v57, v20\n"
    "v_fmac_f32_e32 v140, v58, v20\n"
    "v_fmac_f32_e32 v141, v59, v20\n"
    "v_fmac_f32_e32 v142, v60, v20\n"
    "v_fmac_f32_e32 v143, v61, v20\n"
    "v_fmac_f32_e32 v128, v42, v20\n"
    "v_mfma_f32_16x16x32_bf16 v[38:41], v[92:95], v[144:147], 0\n"
    "v_fmac_f32_e32 v129, v43, v20\n"
    "v_fmac_f32_e32 v130, v44, v20\n"
    "v_fmac_f32_e32 v131, v45, v20\n"
    "v_fmac_f32_e32 v132, v46, v20\n"
    "v_fmac_f32_e32 v133, v47, v20\n"
    "v_fmac_f32_e32 v134, v48, v20\n"
    "v_fmac_f32_e32 v135, v49, v20\n"
    "v_mul_f32_e32 v54, v128, v21\n"
    "v_mul_f32_e32 v55, v129, v21\n"
    "s_add_u32 s23, s16, 118784\n"
    "buffer_load_dwordx4 v[228:231], v18, s[28:31], s23 offen offset:0 sc1\n"
    "s_waitcnt vmcnt(13)\n"
    "v_mfma_f32_16x16x32_bf16 v[34:37], v[104:107], v[148:151], v[34:37]\n"
    "v_max3_u32 v17, v17, v147, v148\n"
    "v_max3_u32 v17, v17, v149, v150\n"
    "v_mul_f32_e32 v56, v130, v21\n"
    "v_mul_f32_e32 v57, v131, v21\n"
    "v_mul_f32_e32 v58, v132, v21\n"
    "v_mul_f32_e32 v59, v133, v21\n"
    "v_mul_f32_e32 v60, v134, v21\n"
    "v_mul_f32_e32 v61, v135, v21\n"
    "v_exp_f32_e32 v54, v54\n"
    "v_mul_f32_e32 v128, v128, v136\n"
    "v_exp_f32_e32 v55, v55\n"
    "v_mfma_f32_16x16x32_bf16 v[38:41], v[108:111], v[148:151], v[38:41]\n"
    "v_mul_f32_e32 v129, v129, v137\n"
    "v_exp_f32_e32 v56, v56\n"
    "v_mul_f32_e32 v130, v130, v138\n"
    "v_exp_f32_e32 v57, v57\n"
    "v_mul_f32_e32 v131, v131, v139\n"
    "v_exp_f32_e32 v58, v58\n"
    "v_mul_f32_e32 v132, v132, v140\n"
    "v_exp_f32_e32 v59, v59\n"
    "v_mul_f32_e32 v133, v133, v141\n"
    "s_add_u32 s23, s16, 120832\n"
    "buffer_load_dwordx4 v[232:235], v18, s[28:31], s23 offen offset:0 sc1\n"
    "v_mfma_f32_16x16x32_bf16 v[26:29], v[80:83], v[144:147], v[26:29]\n"
    "v_exp_f32_e32 v60, v60\n"
    "v_mul_f32_e32 v134, v134, v142\n"
    "v_exp_f32_e32 v61, v61\n"
    "v_mul_f32_e32 v135, v135, v143\n"
    "v_fma_f32 v54, v54, v22, v22\n"
    "v_fma_f32 v55, v55, v22, v22\n"
    "v_fma_f32 v56, v56, v22, v22\n"
    "v_rcp_f32_e32 v54, v54\n"
    "v_fma_f32 v57, v57, v22, v22\n"
    "v_mfma_f32_16x16x32_bf16 v[30:33], v[84:87], v[144:147], v[30:33]\n"
    "v_rcp_f32_e32 v55, v55\n"
    "v_fma_f32 v58, v58, v22, v22\n"
    "v_rcp_f32_e32 v56, v56\n"
    "v_fma_f32 v59, v59, v22, v22\n"
    "v_rcp_f32_e32 v57, v57\n"
    "v_fma_f32 v60, v60, v22, v22\n"
    "v_rcp_f32_e32 v58, v58\n"
    "v_fma_f32 v61, v61, v22, v22\n"
    "v_rcp_f32_e32 v59, v59\n"
    "v_mfma_f32_16x16x32_bf16 v[26:29], v[96:99], v[148:151], v[26:29]\n"
    "v_rcp_f32_e32 v60, v60\n"
    "v_mul_f32_e32 v128, v128, v54\n"
    "v_rcp_f32_e32 v61, v61\n"
    "v_mul_f32_e32 v129, v129, v55\n"
    "v_mul_f32_e32 v130, v130, v56\n"
    "v_mul_f32_e32 v131, v131, v57\n"
    "v_mul_f32_e32 v132, v132, v58\n"
    "v_mul_f32_e32 v133, v133, v59\n"
    "v_mul_f32_e32 v134, v134, v60\n"
    "s_add_u32 s23, s16, 122880\n"
    "buffer_load_dwordx4 v[236:239], v18, s[28:31], s23 offen offset:0 sc1\n"
    "v_mfma_f32_16x16x32_bf16 v[30:33], v[100:103], v[148:151], v[30:33]\n"
    "v_mul_f32_e32 v135, v135, v61\n"
    "v_cvt_pk_bf16_f32 v54, v128, v129\n"
    "v_cvt_pk_bf16_f32 v55, v130, v131\n"
    "v_cvt_pk_bf16_f32 v56, v132, v133\n"
    "v_cvt_pk_bf16_f32 v57, v134, v135\n"
    "v_min_u32_e32 v54, -2, v54\n"
    "v_min_u32_e32 v55, -2, v55\n"
    "v_min_u32_e32 v56, -2, v56\n"
    "v_min_u32_e32 v57, -2, v57\n"
    "s_waitcnt vmcnt(14)\n"
    "v_mfma_f32_16x16x32_bf16 v[34:37], v[120:123], v[152:155], v[34:37]\n"
    "v_max3_u32 v17, v17, v151, v152\n"
    "v_max3_u32 v17, v17, v153, v154\n"
    "buffer_store_dwordx4 v[54:57], v1, s[28:31], s11 offen sc1\n"
    "s_add_u32 s11, s11, 114688\n"
    "s_setprio 0\n"
    ".Lp5p:\n"
    "v_mfma_f32_16x16x32_bf16 v[38:41], v[124:127], v[152:155], v[38:41]\n"
    "s_add_u32 s23, s16, 124928\n"
    "buffer_load_dwordx4 v[240:243], v18, s[28:31], s23 offen offset:0 sc1\n"
    "v_mfma_f32_16x16x32_bf16 v[26:29], v[112:115], v[152:155], v[26:29]\n"
    "v_mfma_f32_16x16x32_bf16 v[30:33], v[116:119], v[152:155], v[30:33]\n"
    "s_waitcnt vmcnt(15)\n"
    "v_mfma_f32_16x16x32_bf16 v[34:37], v[196:199], v[156:159], v[34:37]\n"
    "v_max3_u32 v17, v17, v155, v156\n"
    "v_max3_u32 v17, v17, v157, v158\n"
    "s_add_u32 s23, s16, 126976\n"
    "buffer_load_dwordx4 v[244:247], v18, s[28:31], s23 offen offset:0 sc1\n"
    "v_mfma_f32_16x16x32_bf16 v[38:41], v[200:203], v[156:159], v[38:41]\n"
    "v_mfma_f32_16x16x32_bf16 v[26:29], v[188:191], v[156:159], v[26:29]\n"
    "v_mfma_f32_16x16x32_bf16 v[30:33], v[192:195], v[156:159], v[30:33]\n"
    "s_waitcnt vmcnt(15)\n"
    "v_mfma_f32_16x16x32_bf16 v[34:37], v[180:183], v[160:163], v[34:37]\n"
    "v_max3_u32 v17, v17, v159, v160\n"
    "v_max3_u32 v17, v17, v161, v162\n"
    "v_mfma_f32_16x16x32_bf16 v[38:41], v[184:187], v[160:163], v[38:41]\n"
    "v_mfma_f32_16x16x32_bf16 v[26:29], v[172:175], v[160:163], v[26:29]\n"
    "s_waitcnt vmcnt(9)\n"
    "ds_read_b64_tr_b16 v[248:249], v78 offset:32768\n"
    "ds_read_b64_tr_b16 v[250:251], v78 offset:33792\n"
    "ds_read_b64_tr_b16 v[252:253], v77 offset:32768\n"
    "ds_read_b64_tr_b16 v[254:255], v77 offset:33792\n"
    "v_mfma_f32_16x16x32_bf16 v[30:33], v[176:179], v[160:163], v[30:33]\n"
    "ds_read_b64_tr_b16 v[50:51], v76 offset:32768\n"
    "ds_read_b64_tr_b16 v[52:53], v76 offset:33792\n"
    "ds_read_b64_tr_b16 v[6:7], v75 offset:32768\n"
    "ds_read_b64_tr_b16 v[8:9], v75 offset:33792\n"
    "v_mfma_f32_16x16x32_bf16 v[34:37], v[212:215], v[164:167], v[34:37]\n"
    "v_max3_u32 v17, v17, v163, v164\n"
    "v_max3_u32 v17, v17, v165, v166\n"
    "s_add_u32 s23, s16, 229376\n"
    "buffer_load_dwordx4 v[12:15], v18, s[28:31], s23 offen offset:0 sc1\n"
    "v_mfma_f32_16x16x32_bf16 v[38:41], v[216:219], v[164:167], v[38:41]\n"
    "v_mul_u32_u24_e32 v79, 896, v3\n"
    "v_lshl_add_u32 v79, v2, 3, v79\n"
    "v_mfma_f32_16x16x32_bf16 v[26:29], v[204:207], v[164:167], v[26:29]\n"
    "v_mfma_f32_16x16x32_bf16 v[30:33], v[208:211], v[164:167], v[30:33]\n"
    "s_add_u32 s23, s16, 231424\n"
    "buffer_load_dwordx4 v[42:45], v18, s[28:31], s23 offen offset:0 sc1\n"
    "s_waitcnt lgkmcnt(2)\n"
    "v_mfma_f32_16x16x32_bf16 v[34:37], v[50:53], v[168:171], v[34:37]\n"
    "v_max3_u32 v17, v17, v167, v168\n"
    "v_max3_u32 v17, v17, v169, v170\n"
    "v_max_u32_e32 v17, v17, v171\n"
    "v_cmp_eq_u32_e32 vcc, -1, v17\n"
    "s_waitcnt lgkmcnt(0)\n"
    "v_mfma_f32_16x16x32_bf16 v[38:41], v[6:9], v[168:171], v[38:41]\n"
    "v_mfma_f32_16x16x32_bf16 v[26:29], v[248:251], v[168:171], v[26:29]\n"
    "v_mfma_f32_16x16x32_bf16 v[30:33], v[252:255], v[168:171], v[30:33]\n"
    "s_add_u32 s23, s16, 233472\n"
    "buffer_load_dwordx4 v[46:49], v18, s[28:31], s23 offen offset:0 sc1\n"
    "s_cbranch_vccnz .Lsl0\n"
    ".Lsl0b:\n"
    "ds_write_b128 v65, v[34:37] offset:0\n"
    "s_nop 1\n"
    "ds_write_b128 v66, v[38:41] offset:0\n"
    "s_waitcnt vmcnt(10)\n"
    "v_mfma_f32_16x16x32_bf16 v[34:37], v[88:91], v[220:223], 0\n"
    "v_max3_u32 v17, v220, v221, v222\n"
    "ds_write_b128 v63, v[26:29] offset:0\n"
    "v_mfma_f32_16x16x32_bf16 v[38:41], v[92:95], v[220:223], 0\n"
    "ds_write_b128 v64, v[30:33] offset:0\n"
    "s_waitcnt vmcnt(9)\n"
    "v_mfma_f32_16x16x32_bf16 v[34:37], v[104:107], v[224:227], v[34:37]\n"
    "v_max3_u32 v17, v17, v223, v224\n"
    "v_max3_u32 v17, v17, v225, v226\n"
    "s_cmp_eq_u32 s56, 1\n"
    "s_mov_b32 s52, 0x0c0c0c0c\n"
    "s_cselect_b32 s18, 0x01000c0c, s52\n"
    "s_cselect_b32 s19, 0x03020c0c, s52\n"
    "v_perm_b32 v26, v70, v70, s18\n"
    "v_perm_b32 v27, v70, v70, s19\n"
    "v_perm_b32 v28, v71, v71, s18\n"
    "v_perm_b32 v29, v71, v71, s19\n"
    "v_mfma_f32_16x16x32_bf16 v[38:41], v[108:111], v[224:227], v[38:41]\n"
    "v_perm_b32 v30, v72, v72, s18\n"
    "v_perm_b32 v31, v72, v72, s19\n"
    "v_perm_b32 v32, v73, v73, s18\n"
    "v_perm_b32 v33, v73, v73, s19\n"
    "s_add_u32 s23, s16, 235520\n"
    "buffer_load_dwordx4 v[128:131], v18, s[28:31], s23 offen offset:0 sc1\n"
    "v_mfma_f32_16x16x32_bf16 v[26:29], v[80:83], v[220:223], v[26:29]\n"
    "v_mfma_f32_16x16x32_bf16 v[30:33], v[84:87], v[220:223], v[30:33]\n"
    "v_mfma_f32_16x16x32_bf16 v[26:29], v[96:99], v[224:227], v[26:29]\n"
    "v_mfma_f32_16x16x32_bf16 v[30:33], v[100:103], v[224:227], v[30:33]\n"
    "s_add_u32 s23, s16, 237568\n"
    "buffer_load_dwordx4 v[132:135], v18, s[28:31], s23 offen offset:0 sc1\n"
    "s_waitcnt vmcnt(10)\n"
    "v_mfma_f32_16x16x32_bf16 v[34:37], v[120:123], v[228:231], v[34:37]\n"
    "v_max3_u32 v17, v17, v227, v228\n"
    "v_max3_u32 v17, v17, v229, v230\n"
    "v_mfma_f32_16x16x32_bf16 v[38:41], v[124:127], v[228:231], v[38:41]\n"
    "v_mfma_f32_16x16x32_bf16 v[26:29], v[112:115], v[228:231], v[26:29]\n"
    "v_mfma_f32_16x16x32_bf16 v[30:33], v[116:119], v[228:231], v[30:33]\n"
    "s_add_u32 s23, s16, 239616\n"
    "buffer_load_dwordx4 v[136:139], v18, s[28:31], s23 offen offset:0 sc1\n"
    "s_waitcnt vmcnt(10)\n"
    "v_mfma_f32_16x16x32_bf16 v[34:37], v[196:199], v[232:235], v[34:37]\n"
    "v_max3_u32 v17, v17, v231, v232\n"
    "v_max3_u32 v17, v17, v233, v234\n"
    "v_mfma_f32_16x16x32_bf16 v[38:41], v[200:203], v[232:235], v[38:41]\n"
    "v_mfma_f32_16x16x32_bf16 v[26:29], v[188:191], v[232:235], v[26:29]\n"
    "v_mfma_f32_16x16x32_bf16 v[30:33], v[192:195], v[232:235], v[30:33]\n"
    "s_add_u32 s23, s16, 241664\n"
    "buffer_load_dwordx4 v[140:143], v18, s[28:31], s23 offen offset:0 sc1\n"
    "s_waitcnt vmcnt(10)\n"
    "v_mfma_f32_16x16x32_bf16 v[34:37], v[180:183], v[236:239], v[34:37]\n"
    "v_max3_u32 v17, v17, v235, v236\n"
    "v_max3_u32 v17, v17, v237, v238\n"
    "v_mfma_f32_16x16x32_bf16 v[38:41], v[184:187], v[236:239], v[38:41]\n"
    "v_mfma_f32_16x16x32_bf16 v[26:29], v[172:175], v[236:239], v[26:29]\n"
    "v_mfma_f32_16x16x32_bf16 v[30:33], v[176:179], v[236:239], v[30:33]\n"
    "s_waitcnt vmcnt(8)\n"
    "v_mfma_f32_16x16x32_bf16 v[34:37], v[212:215], v[240:243], v[34:37]\n"
    "v_max3_u32 v17, v17, v239, v240\n"
    "v_max3_u32 v17, v17, v241, v242\n"
    "v_mfma_f32_16x16x32_bf16 v[38:41], v[216:219], v[240:243], v[38:41]\n"
    "v_mfma_f32_16x16x32_bf16 v[26:29], v[204:207], v[240:243], v[26:29]\n"
    "s_add_u32 s23, s16, 344064\n"
    "buffer_load_dwordx4 v[144:147], v18, s[28:31], s23 offen offset:0 sc1\n"
    "v_mfma_f32_16x16x32_bf16 v[30:33], v[208:211], v[240:243], v[30:33]\n"
    "s_waitcnt vmcnt(8)\n"
    "v_mfma_f32_16x16x32_bf16 v[34:37], v[50:53], v[244:247], v[34:37]\n"
    "v_max3_u32 v17, v17, v243, v244\n"
    "v_max3_u32 v17, v17, v245, v246\n"
    "v_max_u32_e32 v17, v17, v247\n"
    "v_cmp_eq_u32_e32 vcc, -1, v17\n"
    "v_mfma_f32_16x16x32_bf16 v[38:41], v[6:9], v[244:247], v[38:41]\n"
    "s_add_u32 s23, s16, 346112\n"
    "buffer_load_dwordx4 v[148:151], v18, s[28:31], s23 offen offset:0 sc1\n"
    "v_mfma_f32_16x16x32_bf16 v[26:29], v[248:251], v[244:247], v[26:29]\n"
    "v_mfma_f32_16x16x32_bf16 v[30:33], v[252:255], v[244:247], v[30:33]\n"
    "s_cbranch_vccnz .Lsl1\n"
    ".Lsl1b:\n"
    "ds_write_b128 v65, v[34:37] offset:32768\n"
    "s_nop 1\n"
    "ds_write_b128 v66, v[38:41] offset:32768\n"
    "s_waitcnt vmcnt(8)\n"
    "v_mfma_f32_16x16x32_bf16 v[34:37], v[88:91], v[12:15], 0\n"
    "v_max3_u32 v17, v12, v13, v14\n"
    "ds_write_b128 v63, v[26:29] offset:32768\n"
    "v_mfma_f32_16x16x32_bf16 v[38:41], v[92:95], v[12:15], 0\n"
    "ds_write_b128 v64, v[30:33] offset:32768\n"
    "s_add_u32 s23, s16, 348160\n"
    "buffer_load_dwordx4 v[152:155], v18, s[28:31], s23 offen offset:0 sc1\n"
    "s_waitcnt vmcnt(8)\n"
    "v_mfma_f32_16x16x32_bf16 v[34:37], v[104:107], v[42:45], v[34:37]\n"
    "v_max3_u32 v17, v17, v15, v42\n"
    "v_max3_u32 v17, v17, v43, v44\n"
    "s_cmp_eq_u32 s56, 2\n"
    "s_mov_b32 s52, 0x0c0c0c0c\n"
    "s_cselect_b32 s18, 0x01000c0c, s52\n"
    "s_cselect_b32 s19, 0x03020c0c, s52\n"
    "v_perm_b32 v26, v70, v70, s18\n"
    "v_perm_b32 v27, v70, v70, s19\n"
    "v_perm_b32 v28, v71, v71, s18\n"
    "v_perm_b32 v29, v71, v71, s19\n"
    "v_mfma_f32_16x16x32_bf16 v[38:41], v[108:111], v[42:45], v[38:41]\n"
    "v_perm_b32 v30, v72, v72, s18\n"
    "v_perm_b32 v31, v72, v72, s19\n"
    "v_perm_b32 v32, v73, v73, s18\n"
    "v_perm_b32 v33, v73, v73, s19\n"
    "v_mfma_f32_16x16x32_bf16 v[26:29], v[80:83], v[12:15], v[26:29]\n"
    "v_add_u32_e32 v63, 0x10000, v63\n"
    "v_add_u32_e32 v64, 0x10000, v64\n"
    "v_add_u32_e32 v65, 0x10000, v65\n"
    "v_add_u32_e32 v66, 0x10000, v66\n"
    "v_mfma_f32_16x16x32_bf16 v[30:33], v[84:87], v[12:15], v[30:33]\n"
    "s_add_u32 s23, s16, 350208\n"
    "buffer_load_dwordx4 v[156:159], v18, s[28:31], s23 offen offset:0 sc1\n"
    "v_mfma_f32_16x16x32_bf16 v[26:29], v[96:99], v[42:45], v[26:29]\n"
    "v_mfma_f32_16x16x32_bf16 v[30:33], v[100:103], v[42:45], v[30:33]\n"
    "s_waitcnt vmcnt(8)\n"
    "v_mfma_f32_16x16x32_bf16 v[34:37], v[120:123], v[46:49], v[34:37]\n"
    "v_max3_u32 v17, v17, v45, v46\n"
    "v_max3_u32 v17, v17, v47, v48\n"
    "v_mfma_f32_16x16x32_bf16 v[38:41], v[124:127], v[46:49], v[38:41]\n"
    "s_add_u32 s23, s16, 352256\n"
    "buffer_load_dwordx4 v[160:163], v18, s[28:31], s23 offen offset:0 sc1\n"
    "v_mfma_f32_16x16x32_bf16 v[26:29], v[112:115], v[46:49], v[26:29]\n"
    "v_mfma_f32_16x16x32_bf16 v[30:33], v[116:119], v[46:49], v[30:33]\n"
    "s_waitcnt vmcnt(8)\n"
    "v_mfma_f32_16x16x32_bf16 v[34:37], v[196:199], v[128:131], v[34:37]\n"
    "v_max3_u32 v17, v17, v49, v128\n"
    "v_max3_u32 v17, v17, v129, v130\n"
    "v_mfma_f32_16x16x32_bf16 v[38:41], v[200:203], v[128:131], v[38:41]\n"
    "s_add_u32 s23, s16, 354304\n"
    "buffer_load_dwordx4 v[164:167], v18, s[28:31], s23 offen offset:0 sc1\n"
    "v_mfma_f32_16x16x32_bf16 v[26:29], v[188:191], v[128:131], v[26:29]\n"
    "v_mfma_f32_16x16x32_bf16 v[30:33], v[192:195], v[128:131], v[30:33]\n"
    "s_waitcnt vmcnt(8)\n"
    "v_mfma_f32_16x16x32_bf16 v[34:37], v[180:183], v[132:135], v[34:37]\n"
    "v_max3_u32 v17, v17, v131, v132\n"
    "v_max3_u32 v17, v17, v133, v134\n"
    "v_mfma_f32_16x16x32_bf16 v[38:41], v[184:187], v[132:135], v[38:41]\n"
    "s_add_u32 s23, s16, 356352\n"
    "buffer_load_dwordx4 v[168:171], v18, s[28:31], s23 offen offset:0 sc1\n"
    "v_mfma_f32_16x16x32_bf16 v[26:29], v[172:175], v[132:135], v[26:29]\n"
    "v_mfma_f32_16x16x32_bf16 v[30:33], v[176:179], v[132:135], v[30:33]\n"
    "s_waitcnt vmcnt(8)\n"
    "v_mfma_f32_16x16x32_bf16 v[34:37], v[212:215], v[136:139], v[34:37]\n"
    "v_max3_u32 v17, v17, v135, v136\n"
    "v_max3_u32 v17, v17, v137, v138\n"
    "v_mfma_f32_16x16x32_bf16 v[38:41], v[216:219], v[136:139], v[38:41]\n"
    "v_mfma_f32_16x16x32_bf16 v[26:29], v[204:207], v[136:139], v[26:29]\n"
    "v_mfma_f32_16x16x32_bf16 v[30:33], v[208:211], v[136:139], v[30:33]\n"
    "s_waitcnt vmcnt(7)\n"
    "v_mfma_f32_16x16x32_bf16 v[34:37], v[50:53], v[140:143], v[34:37]\n"
    "v_max3_u32 v17, v17, v139, v140\n"
    "v_max3_u32 v17, v17, v141, v142\n"
    "v_max_u32_e32 v17, v17, v143\n"
    "v_cmp_eq_u32_e32 vcc, -1, v17\n"
    "v_mfma_f32_16x16x32_bf16 v[38:41], v[6:9], v[140:143], v[38:41]\n"
    "v_mfma_f32_16x16x32_bf16 v[26:29], v[248:251], v[140:143], v[26:29]\n"
    "v_mfma_f32_16x16x32_bf16 v[30:33], v[252:255], v[140:143], v[30:33]\n"
    "s_cbranch_vccnz .Lsl2\n"
    ".Lsl2b:\n"
    "ds_write_b128 v65, v[34:37] offset:0\n"
    "s_nop 3\n"
    "ds_write_b128 v66, v[38:41] offset:0\n"
    "s_waitcnt vmcnt(6)\n"
    "v_mfma_f32_16x16x32_bf16 v[34:37], v[88:91], v[144:147], 0\n"
    "v_max3_u32 v17, v144, v145, v146\n"
    "ds_write_b128 v63, v[26:29] offset:0\n"
    "s_waitcnt lgkmcnt(3)\n"
    "s_barrier\n"
    "ds_read_b128 v[220:223], v10 offset:0\n"
    "ds_read_b128 v[224:227], v10 offset:4096\n"
    "v_mfma_f32_16x16x32_bf16 v[38:41], v[92:95], v[144:147], 0\n"
    "ds_write_b128 v64, v[30:33] offset:0\n"
    "ds_read_b128 v[228:231], v10 offset:8192\n"
    "ds_read_b128 v[232:235], v10 offset:12288\n"
    "s_waitcnt vmcnt(5)\n"
    "v_mfma_f32_16x16x32_bf16 v[34:37], v[104:107], v[148:151], v[34:37]\n"
    "v_max3_u32 v17, v17, v147, v148\n"
    "v_max3_u32 v17, v17, v149, v150\n"
    "s_cmp_eq_u32 s56, 3\n"
    "s_mov_b32 s52, 0x0c0c0c0c\n"
    "s_cselect_b32 s18, 0x01000c0c, s52\n"
    "s_cselect_b32 s19, 0x03020c0c, s52\n"
    "v_perm_b32 v26, v70, v70, s18\n"
    "v_perm_b32 v27, v70, v70, s19\n"
    "v_perm_b32 v28, v71, v71, s18\n"
    "v_perm_b32 v29, v71, v71, s19\n"
    "ds_read_b128 v[236:239], v10 offset:16384\n"
    "v_mfma_f32_16x16x32_bf16 v[38:41], v[108:111], v[148:151], v[38:41]\n"
    "v_perm_b32 v30, v72, v72, s18\n"
    "v_perm_b32 v31, v72, v72, s19\n"
    "v_perm_b32 v32, v73, v73, s18\n"
    "v_perm_b32 v33, v73, v73, s19\n"
    "ds_read_b128 v[240:243], v10 offset:20480\n"
    "ds_read_b128 v[244:247], v10 offset:24576\n"
    "v_mfma_f32_16x16x32_bf16 v[26:29], v[80:83], v[144:147], v[26:29]\n"
    "ds_read_b128 v[58:61], v10 offset:28672\n"
    "v_mfma_f32_16x16x32_bf16 v[30:33], v[84:87], v[144:147], v[30:33]\n"
    "v_add_u32_e32 v10, 0x10000, v10\n"
    "v_mfma_f32_16x16x32_bf16 v[26:29], v[96:99], v[148:151], v[26:29]\n"
    "v_mfma_f32_16x16x32_bf16 v[30:33], v[100:103], v[148:151], v[30:33]\n"
    "s_waitcnt vmcnt(4)\n"
    "v_mfma_f32_16x16x32_bf16 v[34:37], v[120:123], v[152:155], v[34:37]\n"
    "v_max3_u32 v17, v17, v151, v152\n"
    "v_max3_u32 v17, v17, v153, v154\n"
    "s_waitcnt lgkmcnt(7)\n"
    "v_add_f32_e32 v220, v220, v224\n"
    "v_add_f32_e32 v221, v221, v225\n"
    "v_add_f32_e32 v222, v222, v226\n"
    "v_mfma_f32_16x16x32_bf16 v[38:41], v[124:127], v[152:155], v[38:41]\n"
    "v_add_f32_e32 v223, v223, v227\n"
    "s_waitcnt lgkmcnt(4)\n"
    "v_add_f32_e32 v228, v228, v232\n"
    "v_mfma_f32_16x16x32_bf16 v[26:29], v[112:115], v[152:155], v[26:29]\n"
    "v_add_f32_e32 v229, v229, v233\n"
    "v_add_f32_e32 v230, v230, v234\n"
    "v_mfma_f32_16x16x32_bf16 v[30:33], v[116:119], v[152:155], v[30:33]\n"
    "v_add_f32_e32 v231, v231, v235\n"
    "s_waitcnt lgkmcnt(2)\n"
    "v_add_f32_e32 v236, v236, v240\n"
    "v_add_f32_e32 v237, v237, v241\n"
    "s_waitcnt vmcnt(3)\n"
    "v_mfma_f32_16x16x32_bf16 v[34:37], v[196:199], v[156:159], v[34:37]\n"
    "v_max3_u32 v17, v17, v155, v156\n"
    "v_max3_u32 v17, v17, v157, v158\n"
    "v_add_f32_e32 v238, v238, v242\n"
    "v_add_f32_e32 v239, v239, v243\n"
    "v_mfma_f32_16x16x32_bf16 v[38:41], v[200:203], v[156:159], v[38:41]\n"
    "s_waitcnt lgkmcnt(0)\n"
    "v_add_f32_e32 v244, v244, v58\n"
    "v_add_f32_e32 v245, v245, v59\n"
    "v_mfma_f32_16x16x32_bf16 v[26:29], v[188:191], v[156:159], v[26:29]\n"
    "v_add_f32_e32 v246, v246, v60\n"
    "v_add_f32_e32 v247, v247, v61\n"
    "v_mfma_f32_16x16x32_bf16 v[30:33], v[192:195], v[156:159], v[30:33]\n"
    "v_add_f32_e32 v220, v220, v228\n"
    "v_add_f32_e32 v221, v221, v229\n"
    "v_add_f32_e32 v222, v222, v230\n"
    "s_waitcnt vmcnt(2)\n"
    "v_mfma_f32_16x16x32_bf16 v[34:37], v[180:183], v[160:163], v[34:37]\n"
    "v_max3_u32 v17, v17, v159, v160\n"
    "v_max3_u32 v17, v17, v161, v162\n"
    "v_add_f32_e32 v223, v223, v231\n"
    "v_add_f32_e32 v236, v236, v244\n"
    "v_mfma_f32_16x16x32_bf16 v[38:41], v[184:187], v[160:163], v[38:41]\n"
    "v_add_f32_e32 v237, v237, v245\n"
    "v_add_f32_e32 v238, v238, v246\n"
    "v_mfma_f32_16x16x32_bf16 v[26:29], v[172:175], v[160:163], v[26:29]\n"
    "v_add_f32_e32 v239, v239, v247\n"
    "v_add_f32_e32 v220, v220, v236\n"
    "v_add_f32_e32 v221, v221, v237\n"
    "v_mfma_f32_16x16x32_bf16 v[30:33], v[176:179], v[160:163], v[30:33]\n"
    "v_add_f32_e32 v222, v222, v238\n"
    "v_add_f32_e32 v223, v223, v239\n"
    "s_waitcnt vmcnt(1)\n"
    "v_mfma_f32_16x16x32_bf16 v[34:37], v[212:215], v[164:167], v[34:37]\n"
    "v_max3_u32 v17, v17, v163, v164\n"
    "v_max3_u32 v17, v17, v165, v166\n"
    "v_cvt_pk_bf16_f32 v220, v220, v221\n"
    "v_cvt_pk_bf16_f32 v221, v222, v223\n"
    "v_mfma_f32_16x16x32_bf16 v[38:41], v[216:219], v[164:167], v[38:41]\n"
    "buffer_store_dwordx2 v[220:221], v79, s[32:35], s17 offen nt\n"
    "s_add_u32 s17, s17, 57344\n"
    "v_mfma_f32_16x16x32_bf16 v[26:29], v[204:207], v[164:167], v[26:29]\n"
    "v_mfma_f32_16x16x32_bf16 v[30:33], v[208:211], v[164:167], v[30:33]\n"
    "s_waitcnt vmcnt(1)\n"
    "v_mfma_f32_16x16x32_bf16 v[34:37], v[50:53], v[168:171], v[34:37]\n"
    "v_max3_u32 v17, v17, v167, v168\n"
    "v_max3_u32 v17, v17, v169, v170\n"
    "v_max_u32_e32 v17, v17, v171\n"
    "v_cmp_eq_u32_e32 vcc, -1, v17\n"
    "v_mfma_f32_16x16x32_bf16 v[38:41], v[6:9], v[168:171], v[38:41]\n"
    "v_mfma_f32_16x16x32_bf16 v[26:29], v[248:251], v[168:171], v[26:29]\n"
    "v_mfma_f32_16x16x32_bf16 v[30:33], v[252:255], v[168:171], v[30:33]\n"
    "s_cbranch_vccnz .Lsl3\n"
    ".Lsl3b:\n"
    "ds_write_b128 v65, v[34:37] offset:32768\n"
    "s_nop 3\n"
    "ds_write_b128 v66, v[38:41] offset:32768\n"
    "ds_write_b128 v63, v[26:29] offset:32768\n"
    "ds_write_b128 v64, v[30:33] offset:32768\n"
    "s_waitcnt lgkmcnt(0)\n"
    "s_barrier\n"
    "ds_read_b128 v[220:223], v10 offset:0\n"
    "ds_read_b128 v[224:227], v10 offset:4096\n"
    "ds_read_b128 v[228:231], v10 offset:8192\n"
    "ds_read_b128 v[232:235], v10 offset:12288\n"
    "ds_read_b128 v[236:239], v10 offset:16384\n"
    "ds_read_b128 v[240:243], v10 offset:20480\n"
    "ds_read_b128 v[244:247], v10 offset:24576\n"
    "ds_read_b128 v[58:61], v10 offset:28672\n"
    "s_waitcnt lgkmcnt(6)\n"
    "v_add_f32_e32 v220, v220, v224\n"
    "v_add_f32_e32 v221, v221, v225\n"
    "v_add_f32_e32 v222, v222, v226\n"
    "v_add_f32_e32 v223, v223, v227\n"
    "s_waitcnt lgkmcnt(4)\n"
    "v_add_f32_e32 v228, v228, v232\n"
    "v_add_f32_e32 v229, v229, v233\n"
    "v_add_f32_e32 v230, v230, v234\n"
    "v_add_f32_e32 v231, v231, v235\n"
    "s_waitcnt lgkmcnt(2)\n"
    "v_add_f32_e32 v236, v236, v240\n"
    "v_add_f32_e32 v237, v237, v241\n"
    "v_add_f32_e32 v238, v238, v242\n"
    "v_add_f32_e32 v239, v239, v243\n"
    "s_waitcnt lgkmcnt(0)\n"
    "v_add_f32_e32 v244, v244, v58\n"
    "v_add_f32_e32 v245, v245, v59\n"
    "v_add_f32_e32 v246, v246, v60\n"
    "v_add_f32_e32 v247, v247, v61\n"
    "v_add_f32_e32 v220, v220, v228\n"
    "v_add_f32_e32 v221, v221, v229\n"
    "v_add_f32_e32 v222, v222, v230\n"
    "v_add_f32_e32 v223, v223, v231\n"
    "v_add_f32_e32 v236, v236, v244\n"
    "v_add_f32_e32 v237, v237, v245\n"
    "v_add_f32_e32 v238, v238, v246\n"
    "v_add_f32_e32 v239, v239, v247\n"
    "v_add_f32_e32 v220, v220, v236\n"
    "v_add_f32_e32 v221, v221, v237\n"
    "v_add_f32_e32 v222, v222, v238\n"
    "v_add_f32_e32 v223, v223, v239\n"
    "v_cvt_pk_bf16_f32 v220, v220, v221\n"
    "v_cvt_pk_bf16_f32 v221, v222, v223\n"
    "buffer_store_dwordx2 v[220:221], v79, s[32:35], s17 offen nt\n"
    "s_add_u32 s17, s17, 57344\n"
    "s_waitcnt lgkmcnt(0)\n"
    "s_branch .Lfin\n"
    ".Lsl0:\n"
    "s_waitcnt vmcnt(0) lgkmcnt(0)\n"
    "s_nop 15\n"
    ".Lsl0r:\n"
    "s_cmp_eq_u32 s100, 0\n"
    "s_cbranch_scc1 .Lsl0c\n"
    "s_sub_u32 s100, s100, 1\n"
    "s_mov_b64 exec, vcc\n"
    "s_add_u32 s52, s16, 0\n"
    "buffer_load_dwordx4 v[144:147], v18, s[28:31], s52 offen offset:0 sc1\n"
    "s_add_u32 s52, s16, 2048\n"
    "buffer_load_dwordx4 v[148:151], v18, s[28:31], s52 offen offset:0 sc1\n"
    "s_add_u32 s52, s16, 4096\n"
    "buffer_load_dwordx4 v[152:155], v18, s[28:31], s52 offen offset:0 sc1\n"
    "s_add_u32 s52, s16, 6144\n"
    "buffer_load_dwordx4 v[156:159], v18, s[28:31], s52 offen offset:0 sc1\n"
    "s_add_u32 s52, s16, 8192\n"
    "buffer_load_dwordx4 v[160:163], v18, s[28:31], s52 offen offset:0 sc1\n"
    "s_add_u32 s52, s16, 10240\n"
    "buffer_load_dwordx4 v[164:167], v18, s[28:31], s52 offen offset:0 sc1\n"
    "s_add_u32 s52, s16, 12288\n"
    "buffer_load_dwordx4 v[168:171], v18, s[28:31], s52 offen offset:0 sc1\n"
    "s_mov_b64 exec, -1\n"
    "s_waitcnt vmcnt(0)\n"
    "v_max3_u32 v17, v144, v145, v146\n"
    "v_max3_u32 v17, v17, v147, v148\n"
    "v_max3_u32 v17, v17, v149, v150\n"
    "v_max3_u32 v17, v17, v151, v152\n"
    "v_max3_u32 v17, v17, v153, v154\n"
    "v_max3_u32 v17, v17, v155, v156\n"
    "v_max3_u32 v17, v17, v157, v158\n"
    "v_max3_u32 v17, v17, v159, v160\n"
    "v_max3_u32 v17, v17, v161, v162\n"
    "v_max3_u32 v17, v17, v163, v164\n"
    "v_max3_u32 v17, v17, v165, v166\n"
    "v_max3_u32 v17, v17, v167, v168\n"
    "v_max3_u32 v17, v17, v169, v170\n"
    "v_max_u32_e32 v17, v17, v171\n"
    "v_cmp_eq_u32_e32 vcc, -1, v17\n"
    "s_nop 4\n"
    "s_cbranch_vccnz .Lsl0r\n"
    ".Lsl0c:\n"
    "s_cmp_eq_u32 s56, 0\n"
    "s_mov_b32 s52, 0x0c0c0c0c\n"
    "s_cselect_b32 s18, 0x01000c0c, s52\n"
    "s_cselect_b32 s19, 0x03020c0c, s52\n"
    "v_perm_b32 v26, v70, v70, s18\n"
    "v_perm_b32 v27, v70, v70, s19\n"
    "v_perm_b32 v28, v71, v71, s18\n"
    "v_perm_b32 v29, v71, v71, s19\n"
    "v_perm_b32 v30, v72, v72, s18\n"
    "v_perm_b32 v31, v72, v72, s19\n"
    "v_perm_b32 v32, v73, v73, s18\n"
    "v_perm_b32 v33, v73, v73, s19\n"
    "v_mfma_f32_16x16x32_bf16 v[34:37], v[88:91], v[144:147], 0\n"
    "v_mfma_f32_16x16x32_bf16 v[38:41], v[92:95], v[144:147], 0\n"
    "v_mfma_f32_16x16x32_bf16 v[34:37], v[104:107], v[148:151], v[34:37]\n"
    "v_mfma_f32_16x16x32_bf16 v[38:41], v[108:111], v[148:151], v[38:41]\n"
    "v_mfma_f32_16x16x32_bf16 v[26:29], v[80:83], v[144:147], v[26:29]\n"
    "v_mfma_f32_16x16x32_bf16 v[30:33], v[84:87], v[144:147], v[30:33]\n"
    "v_mfma_f32_16x16x32_bf16 v[26:29], v[96:99], v[148:151], v[26:29]\n"
    "v_mfma_f32_16x16x32_bf16 v[30:33], v[100:103], v[148:151], v[30:33]\n"
    "v_mfma_f32_16x16x32_bf16 v[34:37], v[120:123], v[152:155], v[34:37]\n"
    "v_mfma_f32_16x16x32_bf16 v[38:41], v[124:127], v[152:155], v[38:41]\n"
    "v_mfma_f32_16x16x32_bf16 v[26:29], v[112:115], v[152:155], v[26:29]\n"
    "v_mfma_f32_16x16x32_bf16 v[30:33], v[116:119], v[152:155], v[30:33]\n"
    "v_mfma_f32_16x16x32_bf16 v[34:37], v[196:199], v[156:159], v[34:37]\n"
    "v_mfma_f32_16x16x32_bf16 v[38:41], v[200:203], v[156:159], v[38:41]\n"
    "v_mfma_f32_16x16x32_bf16 v[26:29], v[188:191], v[156:159], v[26:29]\n"
    "v_mfma_f32_16x16x32_bf16 v[30:33], v[192:195], v[156:159], v[30:33]\n"
    "v_mfma_f32_16x16x32_bf16 v[34:37], v[180:183], v[160:163], v[34:37]\n"
    "v_mfma_f32_16x16x32_bf16 v[38:41], v[184:187], v[160:163], v[38:41]\n"
    "v_mfma_f32_16x16x32_bf16 v[26:29], v[172:175], v[160:163], v[26:29]\n"
    "v_mfma_f32_16x16x32_bf16 v[30:33], v[176:179], v[160:163], v[30:33]\n"
    "v_mfma_f32_16x16x32_bf16 v[34:37], v[212:215], v[164:167], v[34:37]\n"
    "v_mfma_f32_16x16x32_bf16 v[38:41], v[216:219], v[164:167], v[38:41]\n"
    "v_mfma_f32_16x16x32_bf16 v[26:29], v[204:207], v[164:167], v[26:29]\n"
    "v_mfma_f32_16x16x32_bf16 v[30:33], v[208:211], v[164:167], v[30:33]\n"
    "v_mfma_f32_16x16x32_bf16 v[34:37], v[50:53], v[168:171], v[34:37]\n"
    "v_mfma_f32_16x16x32_bf16 v[38:41], v[6:9], v[168:171], v[38:41]\n"
    "v_mfma_f32_16x16x32_bf16 v[26:29], v[248:251], v[168:171], v[26:29]\n"
    "v_mfma_f32_16x16x32_bf16 v[30:33], v[252:255], v[168:171], v[30:33]\n"
    "s_nop 15\n"
    "s_branch .Lsl0b\n"
    ".Lsl1:\n"
    "s_waitcnt vmcnt(0) lgkmcnt(0)\n"
    "s_nop 15\n"
    ".Lsl1r:\n"
    "s_cmp_eq_u32 s100, 0\n"
    "s_cbranch_scc1 .Lsl1c\n"
    "s_sub_u32 s100, s100, 1\n"
    "s_mov_b64 exec, vcc\n"
    "s_add_u32 s52, s16, 114688\n"
    "buffer_load_dwordx4 v[220:223], v18, s[28:31], s52 offen offset:0 sc1\n"
    "s_add_u32 s52, s16, 116736\n"
    "buffer_load_dwordx4 v[224:227], v18, s[28:31], s52 offen offset:0 sc1\n"
    "s_add_u32 s52, s16, 118784\n"
    "buffer_load_dwordx4 v[228:231], v18, s[28:31], s52 offen offset:0 sc1\n"
    "s_add_u32 s52, s16, 120832\n"
    "buffer_load_dwordx4 v[232:235], v18, s[28:31], s52 offen offset:0 sc1\n"
    "s_add_u32 s52, s16, 122880\n"
    "buffer_load_dwordx4 v[236:239], v18, s[28:31], s52 offen offset:0 sc1\n"
    "s_add_u32 s52, s16, 124928\n"
    "buffer_load_dwordx4 v[240:243], v18, s[28:31], s52 offen offset:0 sc1\n"
    "s_add_u32 s52, s16, 126976\n"
    "buffer_load_dwordx4 v[244:247], v18, s[28:31], s52 offen offset:0 sc1\n"
    "s_mov_b64 exec, -1\n"
    "s_waitcnt vmcnt(0)\n"
    "v_max3_u32 v17, v220, v221, v222\n"
    "v_max3_u32 v17, v17, v223, v224\n"
    "v_max3_u32 v17, v17, v225, v226\n"
    "v_max3_u32 v17, v17, v227, v228\n"
    "v_max3_u32 v17, v17, v229, v230\n"
    "v_max3_u32 v17, v17, v231, v232\n"
    "v_max3_u32 v17, v17, v233, v234\n"
    "v_max3_u32 v17, v17, v235, v236\n"
    "v_max3_u32 v17, v17, v237, v238\n"
    "v_max3_u32 v17, v17, v239, v240\n"
    "v_max3_u32 v17, v17, v241, v242\n"
    "v_max3_u32 v17, v17, v243, v244\n"
    "v_max3_u32 v17, v17, v245, v246\n"
    "v_max_u32_e32 v17, v17, v247\n"
    "v_cmp_eq_u32_e32 vcc, -1, v17\n"
    "s_nop 4\n"
    "s_cbranch_vccnz .Lsl1r\n"
    ".Lsl1c:\n"
    "s_cmp_eq_u32 s56, 1\n"
    "s_mov_b32 s52, 0x0c0c0c0c\n"
    "s_cselect_b32 s18, 0x01000c0c, s52\n"
    "s_cselect_b32 s19, 0x03020c0c, s52\n"
    "v_perm_b32 v26, v70, v70, s18\n"
    "v_perm_b32 v27, v70, v70, s19\n"
    "v_perm_b32 v28, v71, v71, s18\n"
    "v_perm_b32 v29, v71, v71, s19\n"
    "v_perm_b32 v30, v72, v72, s18\n"
    "v_perm_b32 v31, v72, v72, s19\n"
    "v_perm_b32 v32, v73, v73, s18\n"
    "v_perm_b32 v33, v73, v73, s19\n"
    "v_mfma_f32_16x16x32_bf16 v[34:37], v[88:91], v[220:223], 0\n"
    "v_mfma_f32_16x16x32_bf16 v[38:41], v[92:95], v[220:223], 0\n"
    "v_mfma_f32_16x16x32_bf16 v[34:37], v[104:107], v[224:227], v[34:37]\n"
    "v_mfma_f32_16x16x32_bf16 v[38:41], v[108:111], v[224:227], v[38:41]\n"
    "v_mfma_f32_16x16x32_bf16 v[26:29], v[80:83], v[220:223], v[26:29]\n"
    "v_mfma_f32_16x16x32_bf16 v[30:33], v[84:87], v[220:223], v[30:33]\n"
    "v_mfma_f32_16x16x32_bf16 v[26:29], v[96:99], v[224:227], v[26:29]\n"
    "v_mfma_f32_16x16x32_bf16 v[30:33], v[100:103], v[224:227], v[30:33]\n"
    "v_mfma_f32_16x16x32_bf16 v[34:37], v[120:123], v[228:231], v[34:37]\n"
    "v_mfma_f32_16x16x32_bf16 v[38:41], v[124:127], v[228:231], v[38:41]\n"
    "v_mfma_f32_16x16x32_bf16 v[26:29], v[112:115], v[228:231], v[26:29]\n"
    "v_mfma_f32_16x16x32_bf16 v[30:33], v[116:119], v[228:231], v[30:33]\n"
    "v_mfma_f32_16x16x32_bf16 v[34:37], v[196:199], v[232:235], v[34:37]\n"
    "v_mfma_f32_16x16x32_bf16 v[38:41], v[200:203], v[232:235], v[38:41]\n"
    "v_mfma_f32_16x16x32_bf16 v[26:29], v[188:191], v[232:235], v[26:29]\n"
    "v_mfma_f32_16x16x32_bf16 v[30:33], v[192:195], v[232:235], v[30:33]\n"
    "v_mfma_f32_16x16x32_bf16 v[34:37], v[180:183], v[236:239], v[34:37]\n"
    "v_mfma_f32_16x16x32_bf16 v[38:41], v[184:187], v[236:239], v[38:41]\n"
    "v_mfma_f32_16x16x32_bf16 v[26:29], v[172:175], v[236:239], v[26:29]\n"
    "v_mfma_f32_16x16x32_bf16 v[30:33], v[176:179], v[236:239], v[30:33]\n"
    "v_mfma_f32_16x16x32_bf16 v[34:37], v[212:215], v[240:243], v[34:37]\n"
    "v_mfma_f32_16x16x32_bf16 v[38:41], v[216:219], v[240:243], v[38:41]\n"
    "v_mfma_f32_16x16x32_bf16 v[26:29], v[204:207], v[240:243], v[26:29]\n"
    "v_mfma_f32_16x16x32_bf16 v[30:33], v[208:211], v[240:243], v[30:33]\n"
    "v_mfma_f32_16x16x32_bf16 v[34:37], v[50:53], v[244:247], v[34:37]\n"
    "v_mfma_f32_16x16x32_bf16 v[38:41], v[6:9], v[244:247], v[38:41]\n"
    "v_mfma_f32_16x16x32_bf16 v[26:29], v[248:251], v[244:247], v[26:29]\n"
    "v_mfma_f32_16x16x32_bf16 v[30:33], v[252:255], v[244:247], v[30:33]\n"
    "s_nop 15\n"
    "s_branch .Lsl1b\n"
    ".Lsl2:\n"
    "s_waitcnt vmcnt(0) lgkmcnt(0)\n"
    "s_nop 15\n"
    ".Lsl2r:\n"
    "s_cmp_eq_u32 s100, 0\n"
    "s_cbranch_scc1 .Lsl2c\n"
    "s_sub_u32 s100, s100, 1\n"
    "s_mov_b64 exec, vcc\n"
    "s_add_u32 s52, s16, 229376\n"
    "buffer_load_dwordx4 v[12:15], v18, s[28:31], s52 offen offset:0 sc1\n"
    "s_add_u32 s52, s16, 231424\n"
    "buffer_load_dwordx4 v[42:45], v18, s[28:31], s52 offen offset:0 sc1\n"
    "s_add_u32 s52, s16, 233472\n"
    "buffer_load_dwordx4 v[46:49], v18, s[28:31], s52 offen offset:0 sc1\n"
    "s_add_u32 s52, s16, 235520\n"
    "buffer_load_dwordx4 v[128:131], v18, s[28:31], s52 offen offset:0 sc1\n"
    "s_add_u32 s52, s16, 237568\n"
    "buffer_load_dwordx4 v[132:135], v18, s[28:31], s52 offen offset:0 sc1\n"
    "s_add_u32 s52, s16, 239616\n"
    "buffer_load_dwordx4 v[136:139], v18, s[28:31], s52 offen offset:0 sc1\n"
    "s_add_u32 s52, s16, 241664\n"
    "buffer_load_dwordx4 v[140:143], v18, s[28:31], s52 offen offset:0 sc1\n"
    "s_mov_b64 exec, -1\n"
    "s_waitcnt vmcnt(0)\n"
    "v_max3_u32 v17, v12, v13, v14\n"
    "v_max3_u32 v17, v17, v15, v42\n"
    "v_max3_u32 v17, v17, v43, v44\n"
    "v_max3_u32 v17, v17, v45, v46\n"
    "v_max3_u32 v17, v17, v47, v48\n"
    "v_max3_u32 v17, v17, v49, v128\n"
    "v_max3_u32 v17, v17, v129, v130\n"
    "v_max3_u32 v17, v17, v131, v132\n"
    "v_max3_u32 v17, v17, v133, v134\n"
    "v_max3_u32 v17, v17, v135, v136\n"
    "v_max3_u32 v17, v17, v137, v138\n"
    "v_max3_u32 v17, v17, v139, v140\n"
    "v_max3_u32 v17, v17, v141, v142\n"
    "v_max_u32_e32 v17, v17, v143\n"
    "v_cmp_eq_u32_e32 vcc, -1, v17\n"
    "s_nop 4\n"
    "s_cbranch_vccnz .Lsl2r\n"
    ".Lsl2c:\n"
    "s_cmp_eq_u32 s56, 2\n"
    "s_mov_b32 s52, 0x0c0c0c0c\n"
    "s_cselect_b32 s18, 0x01000c0c, s52\n"
    "s_cselect_b32 s19, 0x03020c0c, s52\n"
    "v_perm_b32 v26, v70, v70, s18\n"
    "v_perm_b32 v27, v70, v70, s19\n"
    "v_perm_b32 v28, v71, v71, s18\n"
    "v_perm_b32 v29, v71, v71, s19\n"
    "v_perm_b32 v30, v72, v72, s18\n"
    "v_perm_b32 v31, v72, v72, s19\n"
    "v_perm_b32 v32, v73, v73, s18\n"
    "v_perm_b32 v33, v73, v73, s19\n"
    "v_mfma_f32_16x16x32_bf16 v[34:37], v[88:91], v[12:15], 0\n"
    "v_mfma_f32_16x16x32_bf16 v[38:41], v[92:95], v[12:15], 0\n"
    "v_mfma_f32_16x16x32_bf16 v[34:37], v[104:107], v[42:45], v[34:37]\n"
    "v_mfma_f32_16x16x32_bf16 v[38:41], v[108:111], v[42:45], v[38:41]\n"
    "v_mfma_f32_16x16x32_bf16 v[26:29], v[80:83], v[12:15], v[26:29]\n"
    "v_mfma_f32_16x16x32_bf16 v[30:33], v[84:87], v[12:15], v[30:33]\n"
    "v_mfma_f32_16x16x32_bf16 v[26:29], v[96:99], v[42:45], v[26:29]\n"
    "v_mfma_f32_16x16x32_bf16 v[30:33], v[100:103], v[42:45], v[30:33]\n"
    "v_mfma_f32_16x16x32_bf16 v[34:37], v[120:123], v[46:49], v[34:37]\n"
    "v_mfma_f32_16x16x32_bf16 v[38:41], v[124:127], v[46:49], v[38:41]\n"
    "v_mfma_f32_16x16x32_bf16 v[26:29], v[112:115], v[46:49], v[26:29]\n"
    "v_mfma_f32_16x16x32_bf16 v[30:33], v[116:119], v[46:49], v[30:33]\n"
    "v_mfma_f32_16x16x32_bf16 v[34:37], v[196:199], v[128:131], v[34:37]\n"
    "v_mfma_f32_16x16x32_bf16 v[38:41], v[200:203], v[128:131], v[38:41]\n"
    "v_mfma_f32_16x16x32_bf16 v[26:29], v[188:191], v[128:131], v[26:29]\n"
    "v_mfma_f32_16x16x32_bf16 v[30:33], v[192:195], v[128:131], v[30:33]\n"
    "v_mfma_f32_16x16x32_bf16 v[34:37], v[180:183], v[132:135], v[34:37]\n"
    "v_mfma_f32_16x16x32_bf16 v[38:41], v[184:187], v[132:135], v[38:41]\n"
    "v_mfma_f32_16x16x32_bf16 v[26:29], v[172:175], v[132:135], v[26:29]\n"
    "v_mfma_f32_16x16x32_bf16 v[30:33], v[176:179], v[132:135], v[30:33]\n"
    "v_mfma_f32_16x16x32_bf16 v[34:37], v[212:215], v[136:139], v[34:37]\n"
    "v_mfma_f32_16x16x32_bf16 v[38:41], v[216:219], v[136:139], v[38:41]\n"
    "v_mfma_f32_16x16x32_bf16 v[26:29], v[204:207], v[136:139], v[26:29]\n"
    "v_mfma_f32_16x16x32_bf16 v[30:33], v[208:211], v[136:139], v[30:33]\n"
    "v_mfma_f32_16x16x32_bf16 v[34:37], v[50:53], v[140:143], v[34:37]\n"
    "v_mfma_f32_16x16x32_bf16 v[38:41], v[6:9], v[140:143], v[38:41]\n"
    "v_mfma_f32_16x16x32_bf16 v[26:29], v[248:251], v[140:143], v[26:29]\n"
    "v_mfma_f32_16x16x32_bf16 v[30:33], v[252:255], v[140:143], v[30:33]\n"
    "s_nop 15\n"
    "s_branch .Lsl2b\n"
    ".Lsl3:\n"
    "s_waitcnt vmcnt(0) lgkmcnt(0)\n"
    "s_nop 15\n"
    ".Lsl3r:\n"
    "s_cmp_eq_u32 s100, 0\n"
    "s_cbranch_scc1 .Lsl3c\n"
    "s_sub_u32 s100, s100, 1\n"
    "s_mov_b64 exec, vcc\n"
    "s_add_u32 s52, s16, 344064\n"
    "buffer_load_dwordx4 v[144:147], v18, s[28:31], s52 offen offset:0 sc1\n"
    "s_add_u32 s52, s16, 346112\n"
    "buffer_load_dwordx4 v[148:151], v18, s[28:31], s52 offen offset:0 sc1\n"
    "s_add_u32 s52, s16, 348160\n"
    "buffer_load_dwordx4 v[152:155], v18, s[28:31], s52 offen offset:0 sc1\n"
    "s_add_u32 s52, s16, 350208\n"
    "buffer_load_dwordx4 v[156:159], v18, s[28:31], s52 offen offset:0 sc1\n"
    "s_add_u32 s52, s16, 352256\n"
    "buffer_load_dwordx4 v[160:163], v18, s[28:31], s52 offen offset:0 sc1\n"
    "s_add_u32 s52, s16, 354304\n"
    "buffer_load_dwordx4 v[164:167], v18, s[28:31], s52 offen offset:0 sc1\n"
    "s_add_u32 s52, s16, 356352\n"
    "buffer_load_dwordx4 v[168:171], v18, s[28:31], s52 offen offset:0 sc1\n"
    "s_mov_b64 exec, -1\n"
    "s_waitcnt vmcnt(0)\n"
    "v_max3_u32 v17, v144, v145, v146\n"
    "v_max3_u32 v17, v17, v147, v148\n"
    "v_max3_u32 v17, v17, v149, v150\n"
    "v_max3_u32 v17, v17, v151, v152\n"
    "v_max3_u32 v17, v17, v153, v154\n"
    "v_max3_u32 v17, v17, v155, v156\n"
    "v_max3_u32 v17, v17, v157, v158\n"
    "v_max3_u32 v17, v17, v159, v160\n"
    "v_max3_u32 v17, v17, v161, v162\n"
    "v_max3_u32 v17, v17, v163, v164\n"
    "v_max3_u32 v17, v17, v165, v166\n"
    "v_max3_u32 v17, v17, v167, v168\n"
    "v_max3_u32 v17, v17, v169, v170\n"
    "v_max_u32_e32 v17, v17, v171\n"
    "v_cmp_eq_u32_e32 vcc, -1, v17\n"
    "s_nop 4\n"
    "s_cbranch_vccnz .Lsl3r\n"
    ".Lsl3c:\n"
    "s_cmp_eq_u32 s56, 3\n"
    "s_mov_b32 s52, 0x0c0c0c0c\n"
    "s_cselect_b32 s18, 0x01000c0c, s52\n"
    "s_cselect_b32 s19, 0x03020c0c, s52\n"
    "v_perm_b32 v26, v70, v70, s18\n"
    "v_perm_b32 v27, v70, v70, s19\n"
    "v_perm_b32 v28, v71, v71, s18\n"
    "v_perm_b32 v29, v71, v71, s19\n"
    "v_perm_b32 v30, v72, v72, s18\n"
    "v_perm_b32 v31, v72, v72, s19\n"
    "v_perm_b32 v32, v73, v73, s18\n"
    "v_perm_b32 v33, v73, v73, s19\n"
    "v_mfma_f32_16x16x32_bf16 v[34:37], v[88:91], v[144:147], 0\n"
    "v_mfma_f32_16x16x32_bf16 v[38:41], v[92:95], v[144:147], 0\n"
    "v_mfma_f32_16x16x32_bf16 v[34:37], v[104:107], v[148:151], v[34:37]\n"
    "v_mfma_f32_16x16x32_bf16 v[38:41], v[108:111], v[148:151], v[38:41]\n"
    "v_mfma_f32_16x16x32_bf16 v[26:29], v[80:83], v[144:147], v[26:29]\n"
    "v_mfma_f32_16x16x32_bf16 v[30:33], v[84:87], v[144:147], v[30:33]\n"
    "v_mfma_f32_16x16x32_bf16 v[26:29], v[96:99], v[148:151], v[26:29]\n"
    "v_mfma_f32_16x16x32_bf16 v[30:33], v[100:103], v[148:151], v[30:33]\n"
    "v_mfma_f32_16x16x32_bf16 v[34:37], v[120:123], v[152:155], v[34:37]\n"
    "v_mfma_f32_16x16x32_bf16 v[38:41], v[124:127], v[152:155], v[38:41]\n"
    "v_mfma_f32_16x16x32_bf16 v[26:29], v[112:115], v[152:155], v[26:29]\n"
    "v_mfma_f32_16x16x32_bf16 v[30:33], v[116:119], v[152:155], v[30:33]\n"
    "v_mfma_f32_16x16x32_bf16 v[34:37], v[196:199], v[156:159], v[34:37]\n"
    "v_mfma_f32_16x16x32_bf16 v[38:41], v[200:203], v[156:159], v[38:41]\n"
    "v_mfma_f32_16x16x32_bf16 v[26:29], v[188:191], v[156:159], v[26:29]\n"
    "v_mfma_f32_16x16x32_bf16 v[30:33], v[192:195], v[156:159], v[30:33]\n"
    "v_mfma_f32_16x16x32_bf16 v[34:37], v[180:183], v[160:163], v[34:37]\n"
    "v_mfma_f32_16x16x32_bf16 v[38:41], v[184:187], v[160:163], v[38:41]\n"
    "v_mfma_f32_16x16x32_bf16 v[26:29], v[172:175], v[160:163], v[26:29]\n"
    "v_mfma_f32_16x16x32_bf16 v[30:33], v[176:179], v[160:163], v[30:33]\n"
    "v_mfma_f32_16x16x32_bf16 v[34:37], v[212:215], v[164:167], v[34:37]\n"
    "v_mfma_f32_16x16x32_bf16 v[38:41], v[216:219], v[164:167], v[38:41]\n"
    "v_mfma_f32_16x16x32_bf16 v[26:29], v[204:207], v[164:167], v[26:29]\n"
    "v_mfma_f32_16x16x32_bf16 v[30:33], v[208:211], v[164:167], v[30:33]\n"
    "v_mfma_f32_16x16x32_bf16 v[34:37], v[50:53], v[168:171], v[34:37]\n"
    "v_mfma_f32_16x16x32_bf16 v[38:41], v[6:9], v[168:171], v[38:41]\n"
    "v_mfma_f32_16x16x32_bf16 v[26:29], v[248:251], v[168:171], v[26:29]\n"
    "v_mfma_f32_16x16x32_bf16 v[30:33], v[252:255], v[168:171], v[30:33]\n"
    "s_nop 15\n"
    "s_branch .Lsl3b\n"
    ".Lhelper1:\n"
    "v_mov_b32_e32 v144, -1\n"
    "v_mov_b32_e32 v145, -1\n"
    "v_mov_b32_e32 v146, -1\n"
    "v_mov_b32_e32 v147, -1\n"
    "buffer_store_dwordx4 v[144:147], v1, s[28:31], s9 offen sc1\n"
    "s_add_u32 s9, s9, 16384\n"
    "s_cmp_ge_u32 s9, s10\n"
    "s_cselect_b32 s55, 458752, 0\n"
    "s_sub_u32 s9, s9, s55\n"
    "buffer_store_dwordx4 v[144:147], v1, s[28:31], s9 offen sc1\n"
    "s_add_u32 s9, s9, 16384\n"
    "s_cmp_ge_u32 s9, s10\n"
    "s_cselect_b32 s55, 458752, 0\n"
    "s_sub_u32 s9, s9, s55\n"
    "buffer_store_dwordx4 v[144:147], v1, s[28:31], s9 offen sc1\n"
    "s_add_u32 s9, s9, 16384\n"
    "s_cmp_ge_u32 s9, s10\n"
    "s_cselect_b32 s55, 458752, 0\n"
    "s_sub_u32 s9, s9, s55\n"
    "buffer_store_dwordx4 v[144:147], v1, s[28:31], s9 offen sc1\n"
    "s_add_u32 s9, s9, 16384\n"
    "s_cmp_ge_u32 s9, s10\n"
    "s_cselect_b32 s55, 458752, 0\n"
    "s_sub_u32 s9, s9, s55\n"
    "buffer_store_dwordx4 v[144:147], v1, s[28:31], s9 offen sc1\n"
    "s_add_u32 s9, s9, 16384\n"
    "s_cmp_ge_u32 s9, s10\n"
    "s_cselect_b32 s55, 458752, 0\n"
    "s_sub_u32 s9, s9, s55\n"
    "buffer_store_dwordx4 v[144:147], v1, s[28:31], s9 offen sc1\n"
    "s_add_u32 s9, s9, 16384\n"
    "s_cmp_ge_u32 s9, s10\n"
    "s_cselect_b32 s55, 458752, 0\n"
    "s_sub_u32 s9, s9, s55\n"
    "buffer_store_dwordx4 v[144:147], v1, s[28:31], s9 offen sc1\n"
    "s_add_u32 s9, s9, 16384\n"
    "s_cmp_ge_u32 s9, s10\n"
    "s_cselect_b32 s55, 458752, 0\n"
    "s_sub_u32 s9, s9, s55\n"
    "buffer_store_dwordx4 v[144:147], v1, s[28:31], s9 offen sc1\n"
    "s_add_u32 s9, s9, 16384\n"
    "s_cmp_ge_u32 s9, s10\n"
    "s_cselect_b32 s55, 458752, 0\n"
    "s_sub_u32 s9, s9, s55\n"
    "buffer_store_dwordx4 v[144:147], v1, s[28:31], s9 offen sc1\n"
    "s_add_u32 s9, s9, 16384\n"
    "s_cmp_ge_u32 s9, s10\n"
    "s_cselect_b32 s55, 458752, 0\n"
    "s_sub_u32 s9, s9, s55\n"
    "buffer_store_dwordx4 v[144:147], v1, s[28:31], s9 offen sc1\n"
    "s_add_u32 s9, s9, 16384\n"
    "s_cmp_ge_u32 s9, s10\n"
    "s_cselect_b32 s55, 458752, 0\n"
    "s_sub_u32 s9, s9, s55\n"
    "buffer_store_dwordx4 v[144:147], v1, s[28:31], s9 offen sc1\n"
    "s_add_u32 s9, s9, 16384\n"
    "s_cmp_ge_u32 s9, s10\n"
    "s_cselect_b32 s55, 458752, 0\n"
    "s_sub_u32 s9, s9, s55\n"
    "buffer_store_dwordx4 v[144:147], v1, s[28:31], s9 offen sc1\n"
    "s_add_u32 s9, s9, 16384\n"
    "s_cmp_ge_u32 s9, s10\n"
    "s_cselect_b32 s55, 458752, 0\n"
    "s_sub_u32 s9, s9, s55\n"
    "buffer_store_dwordx4 v[144:147], v1, s[28:31], s9 offen sc1\n"
    "s_add_u32 s9, s9, 16384\n"
    "s_cmp_ge_u32 s9, s10\n"
    "s_cselect_b32 s55, 458752, 0\n"
    "s_sub_u32 s9, s9, s55\n"
    "buffer_store_dwordx4 v[144:147], v1, s[28:31], s9 offen sc1\n"
    "s_add_u32 s9, s9, 16384\n"
    "s_cmp_ge_u32 s9, s10\n"
    "s_cselect_b32 s55, 458752, 0\n"
    "s_sub_u32 s9, s9, s55\n"
    "buffer_store_dwordx4 v[144:147], v1, s[28:31], s9 offen sc1\n"
    "s_add_u32 s9, s9, 16384\n"
    "s_cmp_ge_u32 s9, s10\n"
    "s_cselect_b32 s55, 458752, 0\n"
    "s_sub_u32 s9, s9, s55\n"
    "buffer_store_dwordx4 v[144:147], v1, s[28:31], s9 offen sc1\n"
    "s_add_u32 s9, s9, 16384\n"
    "s_cmp_ge_u32 s9, s10\n"
    "s_cselect_b32 s55, 458752, 0\n"
    "s_sub_u32 s9, s9, s55\n"
    "buffer_store_dwordx4 v[144:147], v1, s[28:31], s9 offen sc1\n"
    "s_add_u32 s9, s9, 16384\n"
    "s_cmp_ge_u32 s9, s10\n"
    "s_cselect_b32 s55, 458752, 0\n"
    "s_sub_u32 s9, s9, s55\n"
    "buffer_store_dwordx4 v[144:147], v1, s[28:31], s9 offen sc1\n"
    "s_add_u32 s9, s9, 16384\n"
    "s_cmp_ge_u32 s9, s10\n"
    "s_cselect_b32 s55, 458752, 0\n"
    "s_sub_u32 s9, s9, s55\n"
    "buffer_store_dwordx4 v[144:147], v1, s[28:31], s9 offen sc1\n"
    "s_add_u32 s9, s9, 16384\n"
    "s_cmp_ge_u32 s9, s10\n"
    "s_cselect_b32 s55, 458752, 0\n"
    "s_sub_u32 s9, s9, s55\n"
    "buffer_store_dwordx4 v[144:147], v1, s[28:31], s9 offen sc1\n"
    "s_add_u32 s9, s9, 16384\n"
    "s_cmp_ge_u32 s9, s10\n"
    "s_cselect_b32 s55, 458752, 0\n"
    "s_sub_u32 s9, s9, s55\n"
    "buffer_store_dwordx4 v[144:147], v1, s[28:31], s9 offen sc1\n"
    "s_add_u32 s9, s9, 16384\n"
    "s_cmp_ge_u32 s9, s10\n"
    "s_cselect_b32 s55, 458752, 0\n"
    "s_sub_u32 s9, s9, s55\n"
    "buffer_store_dwordx4 v[144:147], v1, s[28:31], s9 offen sc1\n"
    "s_add_u32 s9, s9, 16384\n"
    "s_cmp_ge_u32 s9, s10\n"
    "s_cselect_b32 s55, 458752, 0\n"
    "s_sub_u32 s9, s9, s55\n"
    "buffer_store_dwordx4 v[144:147], v1, s[28:31], s9 offen sc1\n"
    "s_add_u32 s9, s9, 16384\n"
    "s_cmp_ge_u32 s9, s10\n"
    "s_cselect_b32 s55, 458752, 0\n"
    "s_sub_u32 s9, s9, s55\n"
    "buffer_store_dwordx4 v[144:147], v1, s[28:31], s9 offen sc1\n"
    "s_add_u32 s9, s9, 16384\n"
    "s_cmp_ge_u32 s9, s10\n"
    "s_cselect_b32 s55, 458752, 0\n"
    "s_sub_u32 s9, s9, s55\n"
    "s_waitcnt vmcnt(0)\n"
    "s_add_u32 s101, s101, 16384\n"
    "s_mov_b64 exec, 1\n"
    "buffer_store_dwordx2 v[4:5], off, s[28:31], s101 sc1\n"
    "s_mov_b64 exec, -1\n"
    "s_cmp_lg_u32 s2, 255\n"
    "s_cbranch_scc1 .Lfin\n"
    "s_lshl_b32 s48, s3, 11\n"
    "s_add_u32 s48, s48, 4096\n"
    "s_mov_b32 s55, 4194304\n"
    ".Lpoll10:\n"
    "buffer_load_dwordx4 v[20:23], v1, s[28:31], s48 offen offset:0 sc1\n"
    "s_sub_u32 s55, s55, 1\n"
    "s_cbranch_scc1 .Lpoll10x\n"
    "s_waitcnt vmcnt(0)\n"
    "v_cmp_ne_u64_e32 vcc, s[36:37], v[20:21]\n"
    "v_cmp_ne_u64_e64 s[52:53], s[36:37], v[22:23]\n"
    "s_nop 0\n"
    "s_or_b64 vcc, vcc, s[52:53]\n"
    "s_cbranch_scc1 .Lpoll10\n"
    ".Lpoll10x:\n"
    "s_waitcnt vmcnt(0)\n"
    "s_mov_b32 s55, 4194304\n"
    ".Lpoll11:\n"
    "buffer_load_dwordx4 v[20:23], v1, s[28:31], s48 offen offset:1024 sc1\n"
    "s_sub_u32 s55, s55, 1\n"
    "s_cbranch_scc1 .Lpoll11x\n"
    "s_waitcnt vmcnt(0)\n"
    "v_cmp_ne_u64_e32 vcc, s[36:37], v[20:21]\n"
    "v_cmp_ne_u64_e64 s[52:53], s[36:37], v[22:23]\n"
    "s_nop 0\n"
    "s_or_b64 vcc, vcc, s[52:53]\n"
    "s_cbranch_scc1 .Lpoll11\n"
    ".Lpoll11x:\n"
    "s_waitcnt vmcnt(0)\n"
    "s_barrier\n"
    "s_cmp_lg_u32 s3, 0\n"
    "s_cbranch_scc1 .Lfin\n"
    "s_mov_b32 s48, 0\n"
    "s_mov_b64 exec, 1\n"
    "buffer_store_dwordx2 v[4:5], off, s[28:31], s48 sc1\n"
    "s_mov_b64 exec, -1\n"
    ".Lfin:\n"
    ".Lmega_end:\n"
    :
    : "{s[0:1]}"(__builtin_amdgcn_kernarg_segment_ptr()), "{s2}"(blockIdx.x), "{v0}"(threadIdx.x)
    : "v1", "v2", "v3", "v4", "v5", "v6", "v7", "v8", "v9", "v10", "v11", "v12", "v13", "v14", "v15", "v16", "v17", "v18", "v19", "v20", "v21", "v22", "v23", "v24", "v25", "v26", "v27", "v28", "v29", "v30", "v31", "v32", "v33", "v34", "v35", "v36", "v37", "v38", "v39", "v40", "v41", "v42", "v43", "v44", "v45", "v46", "v47", "v48", "v49", "v50", "v51", "v52", "v53", "v54", "v55", "v56", "v57", "v58", "v59", "v60", "v61", "v62", "v63", "v64", "v65", "v66", "v67", "v68", "v69", "v70", "v71", "v72", "v73", "v74", "v75", "v76", "v77", "v78", "v79", "v80", "v81", "v82", "v83", "v84", "v85", "v86", "v87", "v88", "v89", "v90", "v91", "v92", "v93", "v94", "v95", "v96", "v97", "v98", "v99", "v100", "v101", "v102", "v103", "v104", "v105", "v106", "v107", "v108", "v109", "v110", "v111", "v112", "v113", "v114", "v115", "v116", "v117", "v118", "v119", "v120", "v121", "v122", "v123", "v124", "v125", "v126", "v127", "v128", "v129", "v130", "v131", "v132", "v133", "v134", "v135", "v136", "v137", "v138", "v139", "v140", "v141", "v142", "v143", "v144", "v145", "v146", "v147", "v148", "v149", "v150", "v151", "v152", "v153", "v154", "v155", "v156", "v157", "v158", "v159", "v160", "v161", "v162", "v163", "v164", "v165", "v166", "v167", "v168", "v169", "v170", "v171", "v172", "v173", "v174", "v175", "v176", "v177", "v178", "v179", "v180", "v181", "v182", "v183", "v184", "v185", "v186", "v187", "v188", "v189", "v190", "v191", "v192", "v193", "v194", "v195", "v196", "v197", "v198", "v199", "v200", "v201", "v202", "v203", "v204", "v205", "v206", "v207", "v208", "v209", "v210", "v211", "v212", "v213", "v214", "v215", "v216", "v217", "v218", "v219", "v220", "v221", "v222", "v223", "v224", "v225", "v226", "v227", "v228", "v229", "v230", "v231", "v232", "v233", "v234", "v235", "v236", "v237", "v238", "v239", "v240", "v241", "v242", "v243", "v244", "v245", "v246", "v247", "v248", "v249", "v250", "v251", "v252", "v253", "v254", "v255", "s3", "s4", "s5", "s6", "s7", "s8", "s9", "s10", "s11", "s12", "s13", "s14", "s15", "s16", "s17", "s18", "s19", "s20", "s21", "s22", "s23", "s24", "s25", "s26", "s27", "s28", "s29", "s30", "s31", "s32", "s33", "s34", "s35", "s36", "s37", "s38", "s39", "s40", "s41", "s42", "s43", "s44", "s45", "s46", "s47", "s48", "s49", "s50", "s51", "s52", "s53", "s54", "s55", "s56", "s57", "s58", "s59", "s60", "s61", "s62", "s63", "s64", "s65", "s66", "s67", "s68", "s69", "s70", "s71", "s72", "s73", "s74", "s75", "s76", "s77", "s78", "s79", "s80", "s81", "s82", "s83", "s84", "s85", "s86", "s87", "s88", "s89", "s90", "s91", "s92", "s93", "s94", "s95", "s96", "s97", "s98", "s99", "s100", "s101", "vcc", "scc", "memory");
}

extern "C" void kernel_launch(void* const* d_in, const int* in_sizes, int n_in, void* d_out, int out_size, void* d_ws, size_t ws_size, hipStream_t stream) {
  (void)hipFuncSetAttribute((const void*)mega, hipFuncAttributeMaxDynamicSharedMemorySize, 163840);
  mega<<<256, 512, 163840, stream>>>(d_in[0], d_in[1], d_in[2], d_in[4], d_in[5], d_in[6], d_ws, d_out);
}
```
